```python
import torch

def _softmax(x, axis=-1):
    return torch.softmax(x, dim=axis)
def _gelu(x):
    return torch.nn.functional.gelu(x, approximate="tanh")
def _silu(x):
    return torch.nn.functional.silu(x)
def _sigmoid(x):
    return torch.sigmoid(x)
def _layernorm(x, axis=-1, eps=1e-5):
    m = x.mean(dim=axis, keepdim=True)
    v = x.var(dim=axis, keepdim=True, unbiased=False)
    return (x - m) / torch.sqrt(v + eps)
def _scatter_add(tgt, idx, src, axis=0):
    return tgt.clone().index_add_(axis, idx, src)
def _segment_sum(src, seg, n):
    out = torch.zeros((n,) + src.shape[1:], dtype=src.dtype)
    return out.index_add_(0, seg, src)
def _pad(x, pw):
    flat = tuple(v for pair in reversed(pw) for v in pair)
    return torch.nn.functional.pad(x, flat)

kernel_name = 'recurrent_scan__gqa_block_cached__swiglu_ffn__decode'

def setup_inputs():

    _w = (lambda a: (lambda t: t.to(torch.bfloat16) if t.is_floating_point() else t)(torch.as_tensor(a)))
    def _A_setup():
        B, Sq, D = 2, 1, 576
        Hq, Hkv, Sk, Dh = 32, 16, 32768, 128
        assert Hq % Hkv == 0, (Hq, Hkv)
        return {
            "x": _w((lambda *s: torch.randn(*s, dtype=torch.bfloat16) * 0.05)(B, Sq, D)),
            "k_cache": _w((lambda *s: torch.randn(*s, dtype=torch.bfloat16) * 1.6)(B, Hkv, Sk, Dh)),
            "v_cache": _w((lambda *s: torch.randn(*s, dtype=torch.bfloat16) * 0.2)(B, Hkv, Sk, Dh)),
            "wq": _w((lambda *s: torch.randn(*s, dtype=torch.bfloat16) * 1.6)(D, Hq * Dh) * (D ** -0.5)),
            "wo": _w((lambda *s: torch.randn(*s, dtype=torch.bfloat16) * 0.2)(Hq * Dh, D) * ((Hq * Dh) ** -0.5)),
        }
    _A_in = _A_setup()

    def _B_setup():
        B, S, D, F = 2, 1, 576, 4608
        return {
            "x": _w((lambda *s: torch.randn(*s, dtype=torch.bfloat16) * 0.05)(B, S, D)),
            "w_gate": _w((lambda *s: torch.randn(*s, dtype=torch.bfloat16) * 0.1)(D, F)),
            "w_up": _w((lambda *s: torch.randn(*s, dtype=torch.bfloat16) * 0.1)(D, F)),
            "w_down": _w((lambda *s: torch.randn(*s, dtype=torch.bfloat16) * 0.1)(F, D)),
        }
    _B_in = _B_setup()

    return {
        "A_x": _A_in["x"],
        "A_k_cache": _A_in["k_cache"],
        "A_v_cache": _A_in["v_cache"],
        "A_wq": _A_in["wq"],
        "A_wo": _A_in["wo"],
        "B_x": _B_in["x"],
        "B_w_gate": _B_in["w_gate"],
        "B_w_up": _B_in["w_up"],
        "B_w_down": _B_in["w_down"],
    }

def reference(A_x, A_k_cache, A_v_cache, A_wq, A_wo, B_x, B_w_gate, B_w_up, B_w_down):
    def _A_ref(x, k_cache, v_cache, wq, wo):
        B, Sq, D = x.shape
        Hq, Dh = 32, 128
        Hkv = k_cache.shape[1]
        rep = Hq // Hkv
        q = (lambda x, *d: x.permute(*d))((x @ wq).reshape(B, Sq, Hq, Dh), 0, 2, 1, 3)
        k = (lambda x, n, axis: torch.repeat_interleave(x, n, dim=axis))(k_cache, rep, axis=1)
        v = (lambda x, n, axis: torch.repeat_interleave(x, n, dim=axis))(v_cache, rep, axis=1)
        scores = torch.einsum("bhqd,bhkd->bhqk", q, k) * (Dh ** -0.5)
        p = _softmax(scores, axis=-1)
        o = torch.einsum("bhqk,bhkd->bhqd", p, v)
        return (lambda x, *d: x.permute(*d))(o, 0, 2, 1, 3).reshape(B, Sq, Hq * Dh) @ wo

    def _B_ref(x, w_gate, w_up, w_down):
        g = _silu(x @ w_gate)
        u = x @ w_up
        return (g * u) @ w_down

    x = A_x
    xn = _layernorm(x, axis=-1)
    s = _A_ref(xn, A_k_cache, A_v_cache, A_wq, A_wo)
    g = _silu(_B_ref(xn, B_w_gate, B_w_up, B_w_down))
    return x + s * g
```

```cpp
#include <hip/hip_runtime.h>
#include <hip/hip_bf16.h>
#include <stdint.h>
#define MIMIC 2
typedef unsigned short u16;
typedef unsigned u32x4 __attribute__((ext_vector_type(4)));
constexpr int B=2, D=576, HQ=32, HKV=16, SK=32768, DH=128, F=4608, NSPLIT=16;
__device__ __forceinline__ float bf2f(u16 x){ return __uint_as_float(((unsigned)x)<<16); }
__device__ __forceinline__ u16 f2bf(float f){ __bf16 b=(__bf16)f; return __builtin_bit_cast(u16,b); }
__device__ __forceinline__ float rnd(float f){ return MIMIC? bf2f(f2bf(f)) : f; }

__device__ void ln_to_lds(const u16* x, float* xn  , float* red){
  int tid=threadIdx.x;
  for(int b=0;b<B;b++){
    float s=0;
    for(int i=tid;i<D;i+=blockDim.x) s+=bf2f(x[b*D+i]);
    for(int o=32;o;o>>=1) s+=__shfl_xor(s,o);
    if((tid&63)==0) red[tid>>6]=s;
    __syncthreads();
    float tot=0; for(int w=0;w<(int)blockDim.x/64;w++) tot+=red[w];
    __syncthreads();
    float mean=tot/D;
    float v=0;
    for(int i=tid;i<D;i+=blockDim.x){ float d=bf2f(x[b*D+i])-mean; v+=d*d; }
    for(int o=32;o;o>>=1) v+=__shfl_xor(v,o);
    if((tid&63)==0) red[tid>>6]=v;
    __syncthreads();
    float vt=0; for(int w=0;w<(int)blockDim.x/64;w++) vt+=red[w];
    __syncthreads();
    float var=rnd(vt/D); float m=rnd(mean);
    float den=rnd(sqrtf(rnd(var+1e-5f)));
    for(int i=tid;i<D;i+=blockDim.x) xn[b*D+i]=rnd(rnd(bf2f(x[b*D+i])-m)/den);
  }
  __syncthreads();
}
__global__ __launch_bounds__(256) void k1(const u16* x,const u16* wq,const u16* wg,const u16* wu,float* q,float* h){
  __shared__ float xn[B*D]; __shared__ float red[4];
  ln_to_lds(x,xn,red);
  int c=blockIdx.x*256+threadIdx.x;
  if(c<HQ*DH){
    float a0=0,a1=0;
    for(int r=0;r<D;r++){ float w=bf2f(wq[(size_t)r*HQ*DH+c]); a0+=xn[r]*w; a1+=xn[D+r]*w; }
    q[c]=rnd(a0); q[HQ*DH+c]=rnd(a1);
  } else {
    c-=HQ*DH;
    float g0=0,g1=0,u0=0,u1=0;
    for(int r=0;r<D;r++){ float w=bf2f(wg[(size_t)r*F+c]); float w2=bf2f(wu[(size_t)r*F+c]); g0+=xn[r]*w; g1+=xn[D+r]*w; u0+=xn[r]*w2; u1+=xn[D+r]*w2; }
    g0=rnd(g0);g1=rnd(g1);u0=rnd(u0);u1=rnd(u1);
    g0=rnd(g0/(1.f+__expf(-g0))); g1=rnd(g1/(1.f+__expf(-g1)));
    h[c]=rnd(g0*u0); h[F+c]=rnd(g1*u1);
  }
}
__global__ __launch_bounds__(256) void k2(const u16* kc,const u16* vc,const float* q,float* pml,float* po){
  int s=blockIdx.x,hk=blockIdx.y,b=blockIdx.z;
  int tid=threadIdx.x,lane=tid&63,w=tid>>6,r=lane>>4,c=lane&15;
  float qf[2][8];
  for(int h=0;h<2;h++) for(int j=0;j<8;j++) qf[h][j]=q[b*HQ*DH+(hk*2+h)*DH+c*8+j];
  const int KPS=SK/NSPLIT;
  size_t base=((size_t)(b*HKV+hk)*SK+(size_t)s*KPS)*DH;
  float m[2]={-INFINITY,-INFINITY},l[2]={0,0},acc[2][8];
  for(int h=0;h<2;h++) for(int j=0;j<8;j++) acc[h][j]=0;
  for(int k=w*4+r;k<KPS;k+=16){
    u32x4 kk=__builtin_nontemporal_load((const u32x4*)(kc+base+(size_t)k*DH+c*8));
    u32x4 vv=__builtin_nontemporal_load((const u32x4*)(vc+base+(size_t)k*DH+c*8));
    float kf[8],vf[8];
    for(int j=0;j<4;j++){ kf[2*j]=__uint_as_float(kk[j]<<16); kf[2*j+1]=__uint_as_float(kk[j]&0xffff0000u); vf[2*j]=__uint_as_float(vv[j]<<16); vf[2*j+1]=__uint_as_float(vv[j]&0xffff0000u);}
    for(int h=0;h<2;h++){
      float d=0; for(int j=0;j<8;j++) d+=kf[j]*qf[h][j];
      d+=__shfl_xor(d,1); d+=__shfl_xor(d,2); d+=__shfl_xor(d,4); d+=__shfl_xor(d,8);
      d=rnd(rnd(d)*0.08838834764831845f);
      float mn=fmaxf(m[h],d); float al=__expf(m[h]-mn), p=__expf(d-mn);
      l[h]=l[h]*al+p; m[h]=mn;
      for(int j=0;j<8;j++) acc[h][j]=acc[h][j]*al+p*vf[j];
    }
  }
  __shared__ float sm[16][2],sl[16][2],sa[16][2][128];
  int g=w*4+r;
  for(int h=0;h<2;h++){ if(c==0){sm[g][h]=m[h];sl[g][h]=l[h];} for(int j=0;j<8;j++) sa[g][h][c*8+j]=acc[h][j]; }
  __syncthreads();
  int h=tid>>7,d=tid&127;
  float M=-INFINITY; for(int i=0;i<16;i++) M=fmaxf(M,sm[i][h]);
  float L=0,o=0; for(int i=0;i<16;i++){ float e=__expf(sm[i][h]-M); L+=e*sl[i][h]; o+=e*sa[i][h][d]; }
  int row=(b*HQ+hk*2+h)*NSPLIT+s;
  po[(size_t)row*DH+d]=o; if(d==0){pml[row*2]=M;pml[row*2+1]=L;}
}
__global__ __launch_bounds__(256) void k3(const u16* x,const u16* wo,const u16* wd,const float* h,const float* pml,const float* po,u16* out){
  __shared__ float o[B][HQ*DH]; __shared__ float red[16][4][16];
  int tid=threadIdx.x;
  for(int i=tid;i<B*HQ*DH;i+=256){
    int row=i>>7,d=i&127;
    float M=-INFINITY; for(int s=0;s<NSPLIT;s++) M=fmaxf(M,pml[(row*NSPLIT+s)*2]);
    float L=0,a=0; for(int s=0;s<NSPLIT;s++){ float e=__expf(pml[(row*NSPLIT+s)*2]-M); L+=e*pml[(row*NSPLIT+s)*2+1]; a+=e*po[(size_t)(row*NSPLIT+s)*DH+d]; }
    (&o[0][0])[i]=rnd(a/L);
  }
  __syncthreads();
  int c=blockIdx.x*16+(tid&15), rg=tid>>4;
  float s0=0,s1=0,y0=0,y1=0;
  for(int r=rg;r<HQ*DH;r+=16){ float w=bf2f(wo[(size_t)r*D+c]); s0+=o[0][r]*w; s1+=o[1][r]*w; }
  for(int r=rg;r<F;r+=16){ float w=bf2f(wd[(size_t)r*D+c]); y0+=h[r]*w; y1+=h[F+r]*w; }
  red[rg][0][tid&15]=s0; red[rg][1][tid&15]=s1; red[rg][2][tid&15]=y0; red[rg][3][tid&15]=y1;
  __syncthreads();
  if(tid<32){
    int b=tid>>4,cc=tid&15; float s=0,y=0;
    for(int i=0;i<16;i++){ s+=red[i][b][cc]; y+=red[i][2+b][cc]; }
    s=rnd(s); y=rnd(y); float g=rnd(y/(1.f+__expf(-y)));
    int col=blockIdx.x*16+cc;
    out[b*D+col]=f2bf(bf2f(x[b*D+col])+rnd(s*g));
  }
}
extern "C" void kernel_launch(void* const* d_in, const int* in_sizes, int n_in, void* d_out, int out_size, void* d_ws, size_t ws_size, hipStream_t stream){
  const u16* x=(const u16*)d_in[0]; const u16* kc=(const u16*)d_in[1]; const u16* vc=(const u16*)d_in[2];
  const u16* wq=(const u16*)d_in[3]; const u16* wo=(const u16*)d_in[4];
  const u16* wg=(const u16*)d_in[6]; const u16* wu=(const u16*)d_in[7]; const u16* wd=(const u16*)d_in[8];
  float* q=(float*)d_ws; float* h=q+B*HQ*DH; float* pml=h+B*F; float* po=pml+B*HQ*NSPLIT*2;
  k1<<<(HQ*DH+F)/256,256,0,stream>>>(x,wq,wg,wu,q,h);
  k2<<<dim3(NSPLIT,HKV,B),256,0,stream>>>(kc,vc,q,pml,po);
  k3<<<D/16,256,0,stream>>>(x,wo,wd,h,pml,po,(u16*)d_out);
}
```

```cpp
#include <hip/hip_runtime.h>
#include <hip/hip_bf16.h>
#include <stdint.h>

typedef unsigned u32;
typedef unsigned short u16;
typedef u32 u32x2 __attribute__((ext_vector_type(2)));
typedef u32 u32x4 __attribute__((ext_vector_type(4)));
typedef float f32x2 __attribute__((ext_vector_type(2)));
typedef float f32x4 __attribute__((ext_vector_type(4)));
typedef __bf16 bf16x8 __attribute__((ext_vector_type(8)));
typedef __bf16 bf16x2 __attribute__((ext_vector_type(2)));
typedef __amdgpu_buffer_rsrc_t rsrc_t;
#define GAS __attribute__((address_space(1)))
#define LAS __attribute__((address_space(3)))

#define NFL 5
#define QPOS 24
#define NAUX 4

constexpr int DM = 576, HQ = 32, SK = 32768, DH = 128, FF = 4608;
constexpr int NWG = 256, NTHR = 512;
constexpr u32 CHUNK = 4096u * DH * 2u;
constexpr int TPW = 32;
constexpr int AUX_NT = 2, AUX_SC1 = 16;
constexpr u32 OOB = 0x80000000u;
constexpr int Q0 = 0, NQ = 64, O0 = 64, NO = 64, F0 = 128, NF = 24, FSUB = 3, Z0 = 152, NZ = 72;
constexpr u32 WS_E = 0, WS_QG = 4096, WS_AG = WS_QG + 32768, AG_REC = 2176, WS_YG = WS_AG + NWG * AG_REC,
              WS_FG = WS_YG + NZ * NO * 128, WS_END = WS_FG + NZ * NF * 128;
constexpr u32 SPIN_CAP = 400000;
constexpr int L_CTR = 0, L_TAG = 4, L_ML = 8, L_ACC = 32, L_XN = L_ACC + 1024, L_RED = L_XN + 4 * 576, L_Y = L_RED + FSUB * 1024,
              L_END = L_Y + 4 * 1280;
constexpr float LOG2E = 1.4426950408889634f;

#define STAMP(k)
#define STAMP_OUT()

__device__ __forceinline__ rsrc_t mkrsrc(const void* p, u32 bytes) { return __builtin_amdgcn_make_buffer_rsrc((void*)p, 0, bytes, 0x00020000); }
__device__ __forceinline__ u32x4 ldnt(rsrc_t r, u32 vo, u32 so) { return __builtin_amdgcn_raw_buffer_load_b128(r, vo, so, AUX_NT); }
__device__ __forceinline__ u32x4 ldsc(rsrc_t r, u32 vo, u32 so) { return __builtin_amdgcn_raw_buffer_load_b128(r, vo, so, AUX_SC1); }
__device__ __forceinline__ void stsc(rsrc_t r, u32 vo, u32x4 v) { __builtin_amdgcn_raw_buffer_store_b128(v, r, vo, 0, AUX_SC1); }
__device__ __forceinline__ f32x4 mfma(u32x4 a, u32x4 b, f32x4 c) {
  return __builtin_amdgcn_mfma_f32_16x16x32_bf16(__builtin_bit_cast(bf16x8, a), __builtin_bit_cast(bf16x8, b), c, 0, 0, 0);
}
__device__ __forceinline__ float asf(u32 x) { return __builtin_bit_cast(float, x); }
__device__ __forceinline__ u32 asu(float x) { return __builtin_bit_cast(u32, x); }
__device__ __forceinline__ u32 pk(float lo, float hi) { bf16x2 v = {(__bf16)lo, (__bf16)hi}; return __builtin_bit_cast(u32, v); }
__device__ __forceinline__ float rbf(float x) { return (float)(__bf16)x; }
__device__ __forceinline__ float lo16(u32 x) { return asf(x << 16); }
__device__ __forceinline__ float hi16(u32 x) { return asf(x & 0xffff0000u); }
template <int CTRL> __device__ __forceinline__ u32 dppu(u32 x) { return (u32)__builtin_amdgcn_mov_dpp((int)x, CTRL, 0xf, 0xf, true); }
template <int CTRL> __device__ __forceinline__ float dppf(float x) { return asf(dppu<CTRL>(asu(x))); }
constexpr int XOR1 = 0xB1, XOR2 = 0x4E, ROR4 = 0x124, ROR8 = 0x128;
__device__ __forceinline__ float sum16(float x) { auto s = __builtin_amdgcn_permlane16_swap(asu(x), asu(x), false, false); return asf(s[0]) + asf(s[1]); }
__device__ __forceinline__ float sum32(float x) { auto s = __builtin_amdgcn_permlane32_swap(asu(x), asu(x), false, false); return asf(s[0]) + asf(s[1]); }
__device__ __forceinline__ float max16(float x) { auto s = __builtin_amdgcn_permlane16_swap(asu(x), asu(x), false, false); return fmaxf(asf(s[0]), asf(s[1])); }
__device__ __forceinline__ float max32(float x) { auto s = __builtin_amdgcn_permlane32_swap(asu(x), asu(x), false, false); return fmaxf(asf(s[0]), asf(s[1])); }
__device__ __forceinline__ float ex2(float x) { return __builtin_amdgcn_exp2f(x); }
__device__ __forceinline__ float silu(float x) { return x / (1.0f + __expf(-x)); }
template <int N> __device__ __forceinline__ void pace() { asm volatile("s_waitcnt vmcnt(%0)" ::"n"(N - 1) : "memory"); }
#define SBAR() __builtin_amdgcn_sched_barrier(0)
#define RELOAD() asm volatile("" ::: "memory")
__device__ __forceinline__ void lds_fence() { asm volatile("s_waitcnt lgkmcnt(0)" ::: "memory"); }
__device__ __forceinline__ void wg_barrier() { lds_fence(); __builtin_amdgcn_s_barrier(); asm volatile("" ::: "memory"); }
template <int N> __device__ __forceinline__ bool tags_ok(const u32x4 (&r)[N], u32 tag) {
  bool bad = false;
#pragma unroll
  for (int i = 0; i < N; ++i) bad |= (r[i][1] != tag) | (r[i][3] != tag);
  return __builtin_amdgcn_ballot_w64(bad) == 0ull;
}
__device__ __forceinline__ u32 next_tag(u32 e) { return e * 1664525u + 1013904223u; }

struct Soft { float m, ml, l; f32x4 acc; };

template <int W, int T> __device__ __forceinline__ u32 bank_mov(u32 old, u32 z) {
  constexpr int ctrl = W == T ? 0xE4 : (W > T ? 0x110 + 4 * (W - T) : 0x100 + 4 * (T - W));
  return (u32)__builtin_amdgcn_update_dpp((int)old, (int)z, ctrl, 0xf, 1 << W, false);
}
template <int T> __device__ __forceinline__ void build_p(u32x4& b, u32 z) {
  b[0] = bank_mov<0, T>(b[0], z); b[1] = bank_mov<1, T>(b[1], z); b[2] = bank_mov<2, T>(b[2], z); b[3] = bank_mov<3, T>(b[3], z);
}
constexpr u32 koffs(int i) { return (i >> 1) * 2048 + (i & 1) * 128; }

template <bool REFILL>
__device__ __forceinline__ void tile(u32x4 (&k)[8], u32x4 (&v)[8], const u32x4 (&bq)[8], u32x4& bp0, u32x4& bp1, Soft& st, bool odd, u32 hm,
                                     rsrc_t rk, rsrc_t rv, u32 ko, u32 vo, u32 so) {
  f32x4 d = {0.f, 0.f, 0.f, 0.f};
#pragma unroll
  for (int i = 0; i < 8; ++i) {
    d = mfma(k[i], bq[i], d);
    SBAR();
    if (REFILL) { pace<NFL>(); k[i] = ldnt(rk, ko, so + koffs(i)); SBAR(); }
  }
  const float a = odd ? d[3] : d[0], bs = odd ? d[1] : d[2];
  float s = a + dppf<XOR1>(bs);
  s = rbf(rbf(s) * 0.08838834764831845f);
  if (__builtin_amdgcn_ballot_w64(s > st.m) != 0ull) {
    float mx = fmaxf(s, dppf<XOR1>(s));
    mx = fmaxf(mx, dppf<ROR4>(mx)); mx = fmaxf(mx, dppf<ROR8>(mx)); mx = max32(max16(mx));
    const float mn = fmaxf(st.m, mx), al = ex2((st.m - mn) * LOG2E);
    st.l *= al; st.acc *= al; st.m = mn; st.ml = mn * LOG2E;
  }
  const float p = ex2(fmaf(s, LOG2E, -st.ml));
  st.l += p;
  const u32 t = pk(p, p);
  const u32 z0 = dppu<0xA0>(t) & hm, z1 = dppu<0xF5>(t) & hm;
#define PV_STEP(U, BP, Z) \
  build_p<(U) / 2>(BP, Z); st.acc = mfma(v[U], BP, st.acc); SBAR(); \
  if (REFILL) { pace<NFL>(); v[U] = ldnt(rv, vo, so + (U) * 1024); SBAR(); }
  PV_STEP(0, bp0, z0) PV_STEP(1, bp1, z1) PV_STEP(2, bp0, z0) PV_STEP(3, bp1, z1)
  PV_STEP(4, bp0, z0) PV_STEP(5, bp1, z1) PV_STEP(6, bp0, z0) PV_STEP(7, bp1, z1)
#undef PV_STEP
}

struct Sel { u32 s[4][2]; };
__device__ __forceinline__ Sel make_sel(int lane) {
  Sel r; const int wp = (lane >> 2) & 3; const bool hb = lane & 1;
#pragma unroll
  for (int par = 0; par < 2; ++par) {
    const u32 two = ((2 * par + 1) << 8) | (2 * par);
    const u32 base = hb ? ((two << 16) | 0x0c0cu) : (0x0c0c0000u | two);
#pragma unroll
    for (int w = 0; w < 4; ++w) r.s[w][par] = wp == w ? base : 0x0c0c0c0cu;
  }
  return r;
}
__device__ __forceinline__ u32x4 build_b(u32 d, const Sel& sel, int par) {
  u32x4 b;
#pragma unroll
  for (int w = 0; w < 4; ++w) b[w] = __builtin_amdgcn_perm(d, d, sel.s[w][par]);
  return b;
}
__device__ __forceinline__ void layernorm(const u32 (&xd)[9], LAS u32* xn, int lane, int aw, u32 (&xm)[18]) {
  float s = 0.f;
#pragma unroll
  for (int k = 0; k < 9; ++k) s += lo16(xd[k]) + hi16(xd[k]);
  s += dppf<XOR1>(s); s += dppf<XOR2>(s); s += dppf<ROR4>(s); s += dppf<ROR8>(s); s = sum16(s);
  const float mean = s * (1.0f / DM);
  float q = 0.f;
#pragma unroll
  for (int k = 0; k < 9; ++k) { const float a = lo16(xd[k]) - mean, b = hi16(xd[k]) - mean; q = fmaf(a, a, q); q = fmaf(b, b, q); }
  q += dppf<XOR1>(q); q += dppf<XOR2>(q); q += dppf<ROR4>(q); q += dppf<ROR8>(q); q = sum16(q);
  const float mb = rbf(mean), den = rbf(sqrtf(rbf(rbf(q * (1.0f / DM)) + 1e-5f))), inv = 1.0f / den;
#pragma unroll
  for (int k = 0; k < 9; ++k) xn[(lane >> 5) * 288 + (lane & 31) + 32 * k] = pk(rbf(lo16(xd[k]) - mb) * inv, rbf(hi16(xd[k]) - mb) * inv);
  lds_fence();
  const LAS u32x2* src = (const LAS u32x2*)(xn + ((lane >> 1) & 1) * 288 + 72 * aw + 18 * (lane >> 4));
#pragma unroll
  for (int k = 0; k < 9; ++k) { const u32x2 t = src[k]; xm[2 * k] = t[0]; xm[2 * k + 1] = t[1]; }
}
__device__ __forceinline__ void aux_sync(LAS u32* ctr, u32 target, int lane) {
  lds_fence();
  if (lane == 0) __hip_atomic_fetch_add(ctr, 1u, __ATOMIC_RELAXED, __HIP_MEMORY_SCOPE_WORKGROUP);
  while (__hip_atomic_load(ctr, __ATOMIC_RELAXED, __HIP_MEMORY_SCOPE_WORKGROUP) < target) __builtin_amdgcn_s_sleep(1);
  asm volatile("" ::: "memory");
}
__device__ __forceinline__ void y_to_lds(const f32x4 (&y)[5], LAS float* ly, int lane, int aw) {
#pragma unroll
  for (int cb = 0; cb < 5; ++cb)
#pragma unroll
    for (int i = 0; i < 4; ++i) ly[aw * 1280 + (16 * cb + 4 * (lane >> 4) + i) * 16 + (lane & 15)] = y[cb][i];
}
template <int NP> __device__ __forceinline__ void y_publish(const LAS float* ly, rsrc_t rw, u32 base, int prod, int t, u32 tag) {
#pragma unroll
  for (int r = 0; r < 3; ++r) {
    const int idx = t + 256 * r;
    if (idx < 576) {
      const LAS f32x2* p = (const LAS f32x2*)(ly + 2 * idx);
      const f32x2 v = (p[0] + p[640]) + (p[1280] + p[1920]);
      stsc(rw, base + ((idx >> 3) * NP + prod) * 128 + (idx & 7) * 16, u32x4{asu(v[0]), tag, asu(v[1]), tag});
    }
  }
}

__global__ void __launch_bounds__(NTHR, 2)
fused_decode(const u16* __restrict__ kc, const u16* __restrict__ vc, unsigned char* ws, const u16* __restrict__ x, const u16* __restrict__ wq,
             const u16* __restrict__ wo, const u16* __restrict__ wg, const u16* __restrict__ wu, const u16* __restrict__ wd, u16* out) {
  __shared__ __attribute__((aligned(16))) u32 smem[L_END];
  LAS u32* lds = (LAS u32*)smem;
  const int lane = threadIdx.x & 63, wave = __builtin_amdgcn_readfirstlane(threadIdx.x >> 6), bid = blockIdx.x;
  const rsrc_t rw = mkrsrc(ws, WS_END);
  STAMP(0);

  if (wave < 4) {
    const rsrc_t rk = mkrsrc((const char*)kc + (size_t)bid * CHUNK, CHUNK), rv = mkrsrc((const char*)vc + (size_t)bid * CHUNK, CHUNK);
    const int g = lane >> 4, rhi = (lane >> 1) & 7;
    const bool odd = lane & 1;
    const u32 ko = (4 * (rhi & 1) + (rhi >> 1)) * 256 + (2 * g + (lane & 1)) * 16, vo = lane * 16;
    u32x4 ka[8], va[8], kb[8], vb[8], qg[4];
    u32 so = wave * 8192, e = 0;
    const u32 qo = WS_QG + ((bid >> 7) * 2048 + ((bid >> 3) & 15) * 128 + ((lane >> 1) & 1) * 64 + 8 * g + 4 * (lane & 1)) * 8;
#pragma unroll
    for (int p = 0; p < 32; ++p) {
      if (p == NFL) {
        e = __builtin_amdgcn_raw_buffer_load_b32(rw, WS_E, 0, AUX_SC1);
        if (wave == 0 && lane < 4) lds[L_CTR + lane] = 0u;
        wg_barrier();
      }
      if (p == QPOS) {
#pragma unroll
        for (int i = 0; i < 4; ++i) qg[i] = ldsc(rw, qo, (i >> 1) * 256 + (i & 1) * 16);
      }
      if (p >= QPOS && p < QPOS + NFL) pace<NFL + 4>();
      else if (p >= NFL && p < 2 * NFL) pace<NFL + 1>();
      else if (p >= NFL) pace<NFL>();
      if (p < 8) ka[p] = ldnt(rk, ko, so + koffs(p));
      else if (p < 16) va[p - 8] = ldnt(rv, vo, so + (p - 8) * 1024);
      else if (p < 24) kb[p - 16] = ldnt(rk, ko, so + 32768 + koffs(p - 16));
      else vb[p - 24] = ldnt(rv, vo, so + 32768 + (p - 24) * 1024);
      SBAR();
    }
    STAMP(1);
    const u32 tag = next_tag(__builtin_amdgcn_readfirstlane(e));
    if (!tags_ok<4>(qg, tag)) {
      u32 spins = SPIN_CAP;
      _Pragma("nounroll") do {
        RELOAD();
#pragma unroll
        for (int i = 0; i < 4; ++i) qg[i] = ldsc(rw, qo, (i >> 1) * 256 + (i & 1) * 16);
      } while (!tags_ok<4>(qg, tag) && --spins);
    }
    STAMP(2);
    u32x4 bq[8];
#pragma unroll
    for (int i = 0; i < 8; ++i) {
      const bool mine = ((lane >> 2) & 3) == (i >> 1);
      const int hf = i & 1;
      bq[i] = u32x4{mine ? qg[2 * hf][0] : 0u, mine ? qg[2 * hf][2] : 0u, mine ? qg[2 * hf + 1][0] : 0u, mine ? qg[2 * hf + 1][2] : 0u};
    }
    Soft st; st.m = -INFINITY; st.ml = -INFINITY; st.l = 0.f; st.acc = f32x4{0.f, 0.f, 0.f, 0.f};
    u32x4 bp0 = {0u, 0u, 0u, 0u}, bp1 = {0u, 0u, 0u, 0u};
    const u32 hm = odd ? 0xffff0000u : 0x0000ffffu;
    for (int it = 0; it < TPW / 2 - 1; ++it) {
      so += 65536;
      tile<true>(ka, va, bq, bp0, bp1, st, odd, hm, rk, rv, ko, vo, so);
      tile<true>(kb, vb, bq, bp0, bp1, st, odd, hm, rk, rv, ko, vo, so + 32768);
      STAMP(8 + it);
    }
    tile<false>(ka, va, bq, bp0, bp1, st, odd, hm, rk, rv, ko, vo, so);
    tile<false>(kb, vb, bq, bp0, bp1, st, odd, hm, rk, rv, ko, vo, so);
    STAMP(3);
    float l = st.l;
    l += dppf<XOR1>(l); l += dppf<ROR4>(l); l += dppf<ROR8>(l); l = sum32(sum16(l));
    LAS float* lf = (LAS float*)lds;
#pragma unroll
    for (int i = 0; i < 4; ++i) lf[L_ACC + (wave * 4 + i) * 64 + lane] = st.acc[i];
    if (lane < 4) lf[L_ML + ((lane >> 1) * 2 + (lane & 1)) * 4 + wave] = odd ? l : st.ml;
    if (lane == 4) lds[L_TAG] = tag;
    wg_barrier();
    STAMP(4);
    if (wave < 2) {
      const f32x4 mw = *(const LAS f32x4*)(lf + L_ML + wave * 8), lw = *(const LAS f32x4*)(lf + L_ML + wave * 8 + 4);
      const float M = fmaxf(fmaxf(mw[0], mw[1]), fmaxf(mw[2], mw[3]));
      float L = 0.f; f32x2 o = {0.f, 0.f};
      const int src = ((lane >> 2) & 3) * 64 + (lane >> 4) * 16 + (lane & 3) * 4 + 2 * wave;
#pragma unroll
      for (int w = 0; w < 4; ++w) {
        const float ew = ex2(mw[w] - M);
        L = fmaf(ew, lw[w], L);
        o += ew * *(const LAS f32x2*)(lf + L_ACC + w * 256 + src);
      }
      const float inv = 1.0f / L;
      const u32 rec = WS_AG + bid * AG_REC;
      stsc(rw, rec + wave * 1024 + lane * 16, u32x4{asu(o[0] * inv), tag, asu(o[1] * inv), tag});
      if (lane == 0) __builtin_amdgcn_raw_buffer_store_b64(u32x2{asu(M + __builtin_amdgcn_logf(L)), tag}, rw, rec + 2048 + wave * 8, 0, AUX_SC1);
    }
    STAMP(5);
    STAMP_OUT();
    return;
  }

  const int aw = wave - 4, g = lane >> 4, c = lane & 15;
  if (bid < Q0 + NQ) {
    const rsrc_t rx = mkrsrc(x, 2 * DM * 2), rq = mkrsrc(wq, DM * HQ * DH * 2);
    u32 xd[9]; u32x4 a[18];
#pragma unroll
    for (int k = 0; k < 9; ++k) xd[k] = __builtin_amdgcn_raw_buffer_load_b32(rx, ((lane >> 5) * 288 + (lane & 31)) * 4, k * 128, 0);
    const u32 e = __builtin_amdgcn_raw_buffer_load_b32(rw, WS_E, 0, AUX_SC1);
    const u32 vo = (144 * aw + 36 * g + (c >> 3)) * 8192 + bid * 128 + (c & 7) * 16;
#pragma unroll
    for (int k = 0; k < 18; ++k) a[k] = ldnt(rq, vo, k * 16384);
    SBAR();
    wg_barrier();
    const Sel sel = make_sel(lane);
    u32 xm[18];
    layernorm(xd, lds + L_XN + aw * 576, lane, aw, xm);
    STAMP(1);
    f32x4 d0 = {0.f, 0.f, 0.f, 0.f}, d1 = d0;
#pragma unroll
    for (int k = 0; k < 18; ++k) { d0 = mfma(a[k], build_b(xm[k], sel, 0), d0); d1 = mfma(a[k], build_b(xm[k], sel, 1), d1); }
    LAS float* red = (LAS float*)lds + L_RED;
#pragma unroll
    for (int i = 0; i < 4; ++i) red[(aw * 4 + i) * 64 + lane] = lane < 32 ? d0[i] : d1[i];
    lds_fence();
    STAMP(2);
    u32 ticket = 0;
    if (lane == 0) ticket = __hip_atomic_fetch_add(lds + L_CTR, 1u, __ATOMIC_RELAXED, __HIP_MEMORY_SCOPE_WORKGROUP);
    if (__builtin_amdgcn_readfirstlane(ticket) == 3u) {
      asm volatile("" ::: "memory");
      const u32 tag = next_tag(__builtin_amdgcn_readfirstlane(e));
      const int bt = lane >> 5, cp = lane & 31, cc = cp >> 2;
      const int src = (cc & 3) * 64 + (cc >> 2) * 16 + (cp & 3) * 4 + 2 * bt;
      f32x2 s = {0.f, 0.f};
#pragma unroll
      for (int w = 0; w < 4; ++w) s += *(const LAS f32x2*)(red + w * 256 + src) + *(const LAS f32x2*)(red + w * 256 + src + 32);
      __builtin_amdgcn_raw_buffer_store_b64(u32x2{pk(s[0], s[1]), tag}, rw, WS_QG + (bt * 2048 + bid * 32 + cp) * 8, 0, AUX_SC1);
    }
    STAMP(3);
    STAMP_OUT();
    return;
  }
  if (bid < O0 + NO) {
    const int uo = bid - O0;
    const rsrc_t ro = mkrsrc(wo, HQ * DH * DM * 2);
    const u32 vo = (64 * uo + 16 * aw + 4 * g) * 1152 + c * 16, vo4 = c < 8 ? vo : OOB;
    u32x4 a[20];
    wg_barrier();
#pragma unroll
    for (int k = 0; k < 20; ++k) {
      if (k >= NAUX) pace<NAUX>();
      a[k] = ldnt(ro, k % 5 == 4 ? vo4 : vo, (k / 5) * 1152 + (k % 5) * 256);
      SBAR();
    }
    const Sel sel = make_sel(lane);
    const int bt = (lane >> 1) & 1, sp = ((lane >> 2) & 3) * 2 + (lane & 1);
    const u32 rec = WS_AG + ((bt * 16 + (uo >> 2)) * 8 + sp) * AG_REC;
    const u32 go = rec + (uo & 3) * 512 + aw * 128 + g * 32, lo = rec + 2048 + ((uo >> 1) & 1) * 8;
    STAMP(1);
    wg_barrier();
    STAMP(2);
    const u32 tag = lds[L_TAG];
    u32x4 g0, g1; u32x2 gl;
    _Pragma("nounroll") for (u32 spins = 0; spins < SPIN_CAP; ++spins) {
      RELOAD();
      g0 = ldsc(rw, go, 0); g1 = ldsc(rw, go, 16); gl = __builtin_amdgcn_raw_buffer_load_b64(rw, lo, 0, AUX_SC1);
      const bool ok = (g0[1] == tag) & (g0[3] == tag) & (g1[1] == tag) & (g1[3] == tag) & (gl[1] == tag);
      if (__builtin_amdgcn_ballot_w64(!ok) == 0ull) break;
    }
    STAMP(3);
    const float lse = asf(gl[0]);
    float M = fmaxf(lse, dppf<XOR1>(lse)); M = fmaxf(M, dppf<ROR4>(M)); M = fmaxf(M, dppf<ROR8>(M));
    const float wt = ex2(lse - M);
    float o[5] = {wt * asf(g0[0]), wt * asf(g0[2]), wt * asf(g1[0]), wt * asf(g1[2]), wt};
#pragma unroll
    for (int i = 0; i < 5; ++i) { o[i] += dppf<XOR1>(o[i]); o[i] += dppf<ROR4>(o[i]); o[i] += dppf<ROR8>(o[i]); }
    const float inv = 1.0f / o[4];
    const u32 om[2] = {pk(o[0] * inv, o[1] * inv), pk(o[2] * inv, o[3] * inv)};
    f32x4 y[5];
#pragma unroll
    for (int cb = 0; cb < 5; ++cb) y[cb] = f32x4{0.f, 0.f, 0.f, 0.f};
#pragma unroll
    for (int s = 0; s < 4; ++s) {
      const u32x4 b = build_b(om[s >> 1], sel, s & 1);
#pragma unroll
      for (int cb = 0; cb < 5; ++cb) y[cb] = mfma(a[s * 5 + cb], b, y[cb]);
    }
    y_to_lds(y, (LAS float*)lds + L_Y, lane, aw);
    wg_barrier();
    STAMP(4);
    y_publish<NO>((LAS float*)lds + L_Y, rw, WS_YG, uo, aw * 64 + lane, tag);
    STAMP(5);
    STAMP_OUT();
    return;
  }
  if (bid < F0 + NF) {
    const int uf = bid - F0;
    const rsrc_t rx = mkrsrc(x, 2 * DM * 2), rd = mkrsrc(wd, FF * DM * 2);
    u32 xd[9];
#pragma unroll
    for (int k = 0; k < 9; ++k) xd[k] = __builtin_amdgcn_raw_buffer_load_b32(rx, ((lane >> 5) * 288 + (lane & 31)) * 4, k * 128, 0);
    const u32 e = __builtin_amdgcn_raw_buffer_load_b32(rw, WS_E, 0, AUX_SC1);
    wg_barrier();
    const Sel sel = make_sel(lane);
    u32 xm[18];
    layernorm(xd, lds + L_XN + aw * 576, lane, aw, xm);
    f32x4 y[5];
#pragma unroll
    for (int cb = 0; cb < 5; ++cb) y[cb] = f32x4{0.f, 0.f, 0.f, 0.f};
    for (int su = 0; su < FSUB; ++su) {
      const int u = uf * FSUB + su;
      const GAS u32x4* gp = (const GAS u32x4*)((const GAS char*)(c < 8 ? wg : wu) + (size_t)(144 * aw + 36 * g) * (FF * 2) + u * 128 + (c & 7) * 16);
      const u32 vo = (64 * u + 16 * aw + 4 * g) * 1152 + c * 16, vo4 = c < 8 ? vo : OOB;
      u32x4 ring[NAUX], a[20];
#pragma unroll
      for (int k = 0; k < NAUX; ++k) ring[k] = __builtin_nontemporal_load(gp + k * (FF * 2 / 16));
      SBAR();
      f32x4 d = {0.f, 0.f, 0.f, 0.f};
#pragma unroll
      for (int k = 0; k < 36; ++k) {
        asm volatile("" : "+v"(xm[k >> 1]));
        d = mfma(ring[k % NAUX], build_b(xm[k >> 1], sel, k & 1), d);
        SBAR();
        if (k + NAUX < 36) ring[k % NAUX] = __builtin_nontemporal_load(gp + (k + NAUX) * (FF * 2 / 16));
        SBAR();
      }
      LAS float* red = (LAS float*)lds + L_RED + su * 1024;
#pragma unroll
      for (int i = 0; i < 4; ++i) red[(aw * 4 + i) * 64 + lane] = d[i];
#pragma unroll
      for (int k = 0; k < 20; ++k) {
        if (k >= NAUX) pace<NAUX>();
        a[k] = ldnt(rd, k % 5 == 4 ? vo4 : vo, (k / 5) * 1152 + (k % 5) * 256);
        SBAR();
      }
      aux_sync(lds + L_CTR, 4 * (su + 1), lane);
      const int cc = 2 * aw + (g >> 1);
      const int src = ((lane >> 2) & 3) * 256 + (cc & 3) * 64 + ((lane & 1) * 2 + (cc >> 2)) * 16 + (g & 1) * 8 + (lane & 2);
      const f32x2 r0 = *(const LAS f32x2*)(red + src), r1 = *(const LAS f32x2*)(red + src + 4);
      float h[4] = {r0[0], r0[1], r1[0], r1[1]};
#pragma unroll
      for (int i = 0; i < 4; ++i) {
        h[i] += dppf<ROR4>(h[i]); h[i] += dppf<ROR8>(h[i]);
        const float own = rbf(h[i]), oth = dppf<XOR1>(own);
        const float gt = (lane & 1) ? oth : own, up = (lane & 1) ? own : oth;
        h[i] = rbf(silu(gt)) * up;
      }
      const u32 hm2[2] = {pk(h[0], h[1]), pk(h[2], h[3])};
#pragma unroll
      for (int s = 0; s < 4; ++s) {
        const u32x4 b = build_b(hm2[s >> 1], sel, s & 1);
#pragma unroll
        for (int cb = 0; cb < 5; ++cb) y[cb] = mfma(a[s * 5 + cb], b, y[cb]);
      }
      STAMP(1 + su);
    }
    y_to_lds(y, (LAS float*)lds + L_Y, lane, aw);
    aux_sync(lds + L_CTR, 4 * (FSUB + 1), lane);
    y_publish<NF>((LAS float*)lds + L_Y, rw, WS_FG, uf, aw * 64 + lane, next_tag(__builtin_amdgcn_readfirstlane(e)));
    STAMP(5);
    STAMP_OUT();
    return;
  }
  if (bid < Z0 + NZ && aw == 0) {
    const int f = bid - Z0, np = lane & 7;
    const u32 oi = (np & 1) * 288 + 4 * f + (np >> 1);
    u32 xv = ((const u32*)x)[oi];
    GAS u32* op = (GAS u32*)out + oi;
    wg_barrier();
    asm volatile("" : "+v"(xv), "+v"(op));
    wg_barrier();
    STAMP(1);
    const u32 tag = lds[L_TAG];
    u32x4 r[8];
    _Pragma("nounroll") for (u32 spins = 0; spins < SPIN_CAP; ++spins) {
      bool ok = true;
      RELOAD();
#pragma unroll
      for (int k = 0; k < 3; ++k) r[k] = ldsc(rw, WS_FG + f * (NF * 128) + lane * 16, k * 1024);
#pragma unroll
      for (int k = 0; k < 3; ++k) ok &= (r[k][1] == tag) & (r[k][3] == tag);
      if (__builtin_amdgcn_ballot_w64(!ok) == 0ull) break;
    }
    float y0 = (asf(r[0][0]) + asf(r[1][0])) + asf(r[2][0]), y1 = (asf(r[0][2]) + asf(r[1][2])) + asf(r[2][2]);
    y0 += dppf<ROR8>(y0); y1 += dppf<ROR8>(y1);
    y0 = sum32(sum16(y0)); y1 = sum32(sum16(y1));
    float g0 = rbf(silu(rbf(y0))), g1 = rbf(silu(rbf(y1)));
    asm volatile("" : "+v"(g0), "+v"(g1));
    STAMP(2);
    _Pragma("nounroll") for (u32 spins = 0; spins < SPIN_CAP; ++spins) {
      bool ok = true;
      RELOAD();
#pragma unroll
      for (int k = 0; k < 8; ++k) r[k] = ldsc(rw, WS_YG + f * (NO * 128) + lane * 16, k * 1024);
#pragma unroll
      for (int k = 0; k < 8; ++k) ok &= (r[k][1] == tag) & (r[k][3] == tag);
      if (__builtin_amdgcn_ballot_w64(!ok) == 0ull) break;
    }
    STAMP(3);
    float s0 = 0.f, s1 = 0.f;
#pragma unroll
    for (int k = 0; k < 8; ++k) { s0 += asf(r[k][0]); s1 += asf(r[k][2]); }
    s0 += dppf<ROR8>(s0); s1 += dppf<ROR8>(s1);
    s0 = sum32(sum16(s0)); s1 = sum32(sum16(s1));
    if (lane < 8) *op = pk(lo16(xv) + rbf(rbf(s0) * g0), hi16(xv) + rbf(rbf(s1) * g1));
    if (f == 0 && lane == 0) __builtin_amdgcn_raw_buffer_store_b32(tag, rw, WS_E, 0, AUX_SC1);
    STAMP(4);
    STAMP_OUT();
    return;
  }
}

extern "C" void kernel_launch(void* const* d_in, const int* in_sizes, int n_in, void* d_out, int out_size, void* d_ws, size_t ws_size,
                              hipStream_t stream) {
  fused_decode<<<NWG, NTHR, 0, stream>>>((const u16*)d_in[1], (const u16*)d_in[2], (unsigned char*)d_ws, (const u16*)d_in[0], (const u16*)d_in[3],
                                          (const u16*)d_in[4], (const u16*)d_in[6], (const u16*)d_in[7], (const u16*)d_in[8], (u16*)d_out);
}
```

```cpp
#define PROBE 0
#include <hip/hip_runtime.h>
#include <hip/hip_bf16.h>
#include <stdint.h>

typedef unsigned u32;
typedef unsigned short u16;
typedef u32 u32x2 __attribute__((ext_vector_type(2)));
typedef u32 u32x4 __attribute__((ext_vector_type(4)));
typedef float f32x2 __attribute__((ext_vector_type(2)));
typedef float f32x4 __attribute__((ext_vector_type(4)));
typedef __bf16 bf16x8 __attribute__((ext_vector_type(8)));
typedef __bf16 bf16x2 __attribute__((ext_vector_type(2)));
typedef __amdgpu_buffer_rsrc_t rsrc_t;
#define GAS __attribute__((address_space(1)))
#define LAS __attribute__((address_space(3)))

#define NFL 5
#define QPOS 24
#define NAUX 4

constexpr int DM = 576, HQ = 32, SK = 32768, DH = 128, FF = 4608;
constexpr int NWG = 256, NTHR = 512;
constexpr u32 CHUNK = 4096u * DH * 2u;
constexpr int TPW = 32;
constexpr int AUX_NT = 2, AUX_SC1 = 16;
constexpr u32 OOB = 0x80000000u;
constexpr int Q0 = 0, NQ = 64, O0 = 64, NO = 64, F0 = 128, NF = 24, FSUB = 3, Z0 = 152, NZ = 72;
constexpr u32 WS_E = 0, WS_QG = 4096, WS_AG = WS_QG + 32768, AG_REC = 2176, WS_YG = WS_AG + NWG * AG_REC,
              WS_FG = WS_YG + NZ * NO * 128, WS_END = WS_FG + NZ * NF * 128;
constexpr u32 SPIN_CAP = 400000;
constexpr int L_CTR = 0, L_TAG = 4, L_ML = 8, L_ACC = 32, L_XN = L_ACC + 1024, L_RED = L_XN + 4 * 576, L_Y = L_RED + FSUB * 1024,
              L_END = L_Y + 4 * 1280;
constexpr float LOG2E = 1.4426950408889634f;

#define STAMP(k)
#define STAMP_OUT()

__device__ __forceinline__ rsrc_t mkrsrc(const void* p, u32 bytes) { return __builtin_amdgcn_make_buffer_rsrc((void*)p, 0, bytes, 0x00020000); }
__device__ __forceinline__ u32x4 ldnt(rsrc_t r, u32 vo, u32 so) { return __builtin_amdgcn_raw_buffer_load_b128(r, vo, so, AUX_NT); }
__device__ __forceinline__ u32x4 ldsc(rsrc_t r, u32 vo, u32 so) { return __builtin_amdgcn_raw_buffer_load_b128(r, vo, so, AUX_SC1); }
__device__ __forceinline__ void stsc(rsrc_t r, u32 vo, u32x4 v) { __builtin_amdgcn_raw_buffer_store_b128(v, r, vo, 0, AUX_SC1); }
__device__ __forceinline__ f32x4 mfma(u32x4 a, u32x4 b, f32x4 c) {
  return __builtin_amdgcn_mfma_f32_16x16x32_bf16(__builtin_bit_cast(bf16x8, a), __builtin_bit_cast(bf16x8, b), c, 0, 0, 0);
}
__device__ __forceinline__ float asf(u32 x) { return __builtin_bit_cast(float, x); }
__device__ __forceinline__ u32 asu(float x) { return __builtin_bit_cast(u32, x); }
__device__ __forceinline__ u32 pk(float lo, float hi) { bf16x2 v = {(__bf16)lo, (__bf16)hi}; return __builtin_bit_cast(u32, v); }
__device__ __forceinline__ float rbf(float x) { return (float)(__bf16)x; }
__device__ __forceinline__ float lo16(u32 x) { return asf(x << 16); }
__device__ __forceinline__ float hi16(u32 x) { return asf(x & 0xffff0000u); }
template <int CTRL> __device__ __forceinline__ u32 dppu(u32 x) { return (u32)__builtin_amdgcn_mov_dpp((int)x, CTRL, 0xf, 0xf, true); }
template <int CTRL> __device__ __forceinline__ float dppf(float x) { return asf(dppu<CTRL>(asu(x))); }
constexpr int XOR1 = 0xB1, XOR2 = 0x4E, ROR4 = 0x124, ROR8 = 0x128;
__device__ __forceinline__ float sum16(float x) { auto s = __builtin_amdgcn_permlane16_swap(asu(x), asu(x), false, false); return asf(s[0]) + asf(s[1]); }
__device__ __forceinline__ float sum32(float x) { auto s = __builtin_amdgcn_permlane32_swap(asu(x), asu(x), false, false); return asf(s[0]) + asf(s[1]); }
__device__ __forceinline__ float max16(float x) { auto s = __builtin_amdgcn_permlane16_swap(asu(x), asu(x), false, false); return fmaxf(asf(s[0]), asf(s[1])); }
__device__ __forceinline__ float max32(float x) { auto s = __builtin_amdgcn_permlane32_swap(asu(x), asu(x), false, false); return fmaxf(asf(s[0]), asf(s[1])); }
__device__ __forceinline__ float ex2(float x) { return __builtin_amdgcn_exp2f(x); }
__device__ __forceinline__ float silu(float x) { return x / (1.0f + __expf(-x)); }
template <int N> __device__ __forceinline__ void pace() { asm volatile("s_waitcnt vmcnt(%0)" ::"n"(N - 1) : "memory"); }
#define SBAR() __builtin_amdgcn_sched_barrier(0)
#define RELOAD() asm volatile("" ::: "memory")
__device__ __forceinline__ void lds_fence() { asm volatile("s_waitcnt lgkmcnt(0)" ::: "memory"); }
__device__ __forceinline__ void wg_barrier() { lds_fence(); __builtin_amdgcn_s_barrier(); asm volatile("" ::: "memory"); }
template <int N> __device__ __forceinline__ bool tags_ok(const u32x4 (&r)[N], u32 tag) {
  bool bad = false;
#pragma unroll
  for (int i = 0; i < N; ++i) bad |= (r[i][1] != tag) | (r[i][3] != tag);
  return __builtin_amdgcn_ballot_w64(bad) == 0ull;
}
__device__ __forceinline__ u32 next_tag(u32 e) { return e * 1664525u + 1013904223u; }

struct Soft { float m, ml, l; f32x4 acc; };

template <int W, int T> __device__ __forceinline__ u32 bank_mov(u32 old, u32 z) {
  constexpr int ctrl = W == T ? 0xE4 : (W > T ? 0x110 + 4 * (W - T) : 0x100 + 4 * (T - W));
  return (u32)__builtin_amdgcn_update_dpp((int)old, (int)z, ctrl, 0xf, 1 << W, false);
}
template <int T> __device__ __forceinline__ void build_p(u32x4& b, u32 z) {
  b[0] = bank_mov<0, T>(b[0], z); b[1] = bank_mov<1, T>(b[1], z); b[2] = bank_mov<2, T>(b[2], z); b[3] = bank_mov<3, T>(b[3], z);
}
constexpr u32 koffs(int i) { return (i >> 1) * 2048 + (i & 1) * 128; }

template <bool REFILL>
__device__ __forceinline__ void tile(u32x4 (&k)[8], u32x4 (&v)[8], const u32x4 (&bq)[8], u32x4& bp0, u32x4& bp1, Soft& st, bool odd, u32 hm,
                                     rsrc_t rk, rsrc_t rv, u32 ko, u32 vo, u32 so) {
  f32x4 d = {0.f, 0.f, 0.f, 0.f};
#pragma unroll
  for (int i = 0; i < 8; ++i) {
    d = mfma(k[i], bq[i], d);
    SBAR();
    if (REFILL) { pace<NFL>(); k[i] = ldnt(rk, ko, so + koffs(i)); SBAR(); }
  }
  const float a = odd ? d[3] : d[0], bs = odd ? d[1] : d[2];
  float s = a + dppf<XOR1>(bs);
  s = rbf(rbf(s) * 0.08838834764831845f);
  if (__builtin_amdgcn_ballot_w64(s > st.m) != 0ull) {
    float mx = fmaxf(s, dppf<XOR1>(s));
    mx = fmaxf(mx, dppf<ROR4>(mx)); mx = fmaxf(mx, dppf<ROR8>(mx)); mx = max32(max16(mx));
    const float mn = fmaxf(st.m, mx), al = ex2((st.m - mn) * LOG2E);
    st.l *= al; st.acc *= al; st.m = mn; st.ml = mn * LOG2E;
  }
  const float p = ex2(fmaf(s, LOG2E, -st.ml));
  st.l += p;
  const u32 t = pk(p, p);
  const u32 z0 = dppu<0xA0>(t) & hm, z1 = dppu<0xF5>(t) & hm;
#define PV_STEP(U, BP, Z) \
  build_p<(U) / 2>(BP, Z); st.acc = mfma(v[U], BP, st.acc); SBAR(); \
  if (REFILL) { pace<NFL>(); v[U] = ldnt(rv, vo, so + (U) * 1024); SBAR(); }
  PV_STEP(0, bp0, z0) PV_STEP(1, bp1, z1) PV_STEP(2, bp0, z0) PV_STEP(3, bp1, z1)
  PV_STEP(4, bp0, z0) PV_STEP(5, bp1, z1) PV_STEP(6, bp0, z0) PV_STEP(7, bp1, z1)
#undef PV_STEP
}

struct Sel { u32 s[4][2]; };
__device__ __forceinline__ Sel make_sel(int lane) {
  Sel r; const int wp = (lane >> 2) & 3; const bool hb = lane & 1;
#pragma unroll
  for (int par = 0; par < 2; ++par) {
    const u32 two = ((2 * par + 1) << 8) | (2 * par);
    const u32 base = hb ? ((two << 16) | 0x0c0cu) : (0x0c0c0000u | two);
#pragma unroll
    for (int w = 0; w < 4; ++w) r.s[w][par] = wp == w ? base : 0x0c0c0c0cu;
  }
  return r;
}
__device__ __forceinline__ u32x4 build_b(u32 d, const Sel& sel, int par) {
  u32x4 b;
#pragma unroll
  for (int w = 0; w < 4; ++w) b[w] = __builtin_amdgcn_perm(d, d, sel.s[w][par]);
  return b;
}
__device__ __forceinline__ void layernorm(const u32 (&xd)[9], LAS u32* xn, int lane, int aw, u32 (&xm)[18]) {
  float s = 0.f;
#pragma unroll
  for (int k = 0; k < 9; ++k) s += lo16(xd[k]) + hi16(xd[k]);
  s += dppf<XOR1>(s); s += dppf<XOR2>(s); s += dppf<ROR4>(s); s += dppf<ROR8>(s); s = sum16(s);
  const float mean = s * (1.0f / DM);
  float q = 0.f;
#pragma unroll
  for (int k = 0; k < 9; ++k) { const float a = lo16(xd[k]) - mean, b = hi16(xd[k]) - mean; q = fmaf(a, a, q); q = fmaf(b, b, q); }
  q += dppf<XOR1>(q); q += dppf<XOR2>(q); q += dppf<ROR4>(q); q += dppf<ROR8>(q); q = sum16(q);
  const float mb = rbf(mean), den = rbf(sqrtf(rbf(rbf(q * (1.0f / DM)) + 1e-5f))), inv = 1.0f / den;
#pragma unroll
  for (int k = 0; k < 9; ++k) xn[(lane >> 5) * 288 + (lane & 31) + 32 * k] = pk(rbf(lo16(xd[k]) - mb) * inv, rbf(hi16(xd[k]) - mb) * inv);
  lds_fence();
  const LAS u32x2* src = (const LAS u32x2*)(xn + ((lane >> 1) & 1) * 288 + 72 * aw + 18 * (lane >> 4));
#pragma unroll
  for (int k = 0; k < 9; ++k) { const u32x2 t = src[k]; xm[2 * k] = t[0]; xm[2 * k + 1] = t[1]; }
}
__device__ __forceinline__ void aux_sync(LAS u32* ctr, u32 target, int lane) {
  lds_fence();
  if (lane == 0) __hip_atomic_fetch_add(ctr, 1u, __ATOMIC_RELAXED, __HIP_MEMORY_SCOPE_WORKGROUP);
  while (__hip_atomic_load(ctr, __ATOMIC_RELAXED, __HIP_MEMORY_SCOPE_WORKGROUP) < target) __builtin_amdgcn_s_sleep(1);
  asm volatile("" ::: "memory");
}
__device__ __forceinline__ void y_to_lds(const f32x4 (&y)[5], LAS float* ly, int lane, int aw) {
#pragma unroll
  for (int cb = 0; cb < 5; ++cb)
#pragma unroll
    for (int i = 0; i < 4; ++i) ly[aw * 1280 + (16 * cb + 4 * (lane >> 4) + i) * 16 + (lane & 15)] = y[cb][i];
}
template <int NP> __device__ __forceinline__ void y_publish(const LAS float* ly, rsrc_t rw, u32 base, int prod, int t, u32 tag) {
#pragma unroll
  for (int r = 0; r < 3; ++r) {
    const int idx = t + 256 * r;
    if (idx < 576) {
      const LAS f32x2* p = (const LAS f32x2*)(ly + 2 * idx);
      const f32x2 v = (p[0] + p[640]) + (p[1280] + p[1920]);
      stsc(rw, base + ((idx >> 3) * NP + prod) * 128 + (idx & 7) * 16, u32x4{asu(v[0]), tag, asu(v[1]), tag});
    }
  }
}

__global__ void __launch_bounds__(NTHR, 2)
fused_decode(const u16* __restrict__ kc, const u16* __restrict__ vc, unsigned char* ws, const u16* __restrict__ x, const u16* __restrict__ wq,
             const u16* __restrict__ wo, const u16* __restrict__ wg, const u16* __restrict__ wu, const u16* __restrict__ wd, u16* out) {
  __shared__ __attribute__((aligned(16))) u32 smem[L_END];
  LAS u32* lds = (LAS u32*)smem;
  const int lane = threadIdx.x & 63, wave = __builtin_amdgcn_readfirstlane(threadIdx.x >> 6), bid = blockIdx.x;
  const rsrc_t rw = mkrsrc(ws, WS_END);
  STAMP(0);
  const u32 tstart = (u32)__builtin_amdgcn_s_memrealtime();
  u32 probe = 0;
#define PROBE_AT(k) do { if (PROBE == (k)) probe = (u32)__builtin_amdgcn_s_memrealtime() - tstart; } while (0)

  if (wave < 4) {
    const rsrc_t rk = mkrsrc((const char*)kc + (size_t)bid * CHUNK, CHUNK), rv = mkrsrc((const char*)vc + (size_t)bid * CHUNK, CHUNK);
    const int g = lane >> 4, rhi = (lane >> 1) & 7;
    const bool odd = lane & 1;
    const u32 ko = (4 * (rhi & 1) + (rhi >> 1)) * 256 + (2 * g + (lane & 1)) * 16, vo = lane * 16;
    u32x4 ka[8], va[8], kb[8], vb[8], qg[4];
    u32 so = wave * 8192, e = 0;
    const u32 qo = WS_QG + ((bid >> 7) * 2048 + ((bid >> 3) & 15) * 128 + ((lane >> 1) & 1) * 64 + 8 * g + 4 * (lane & 1)) * 8;
#pragma unroll
    for (int p = 0; p < 32; ++p) {
      if (p == NFL) {
        e = __builtin_amdgcn_raw_buffer_load_b32(rw, WS_E, 0, AUX_SC1);
        if (wave == 0 && lane < 4) lds[L_CTR + lane] = 0u;
        wg_barrier();
      }
      if (p == QPOS) {
#pragma unroll
        for (int i = 0; i < 4; ++i) qg[i] = ldsc(rw, qo, (i >> 1) * 256 + (i & 1) * 16);
      }
      if (p >= QPOS && p < QPOS + NFL) pace<NFL + 4>();
      else if (p >= NFL && p < 2 * NFL) pace<NFL + 1>();
      else if (p >= NFL) pace<NFL>();
      if (p < 8) ka[p] = ldnt(rk, ko, so + koffs(p));
      else if (p < 16) va[p - 8] = ldnt(rv, vo, so + (p - 8) * 1024);
      else if (p < 24) kb[p - 16] = ldnt(rk, ko, so + 32768 + koffs(p - 16));
      else vb[p - 24] = ldnt(rv, vo, so + 32768 + (p - 24) * 1024);
      SBAR();
    }
    STAMP(1); PROBE_AT(1);
    const u32 tag = next_tag(__builtin_amdgcn_readfirstlane(e));
    if (!tags_ok<4>(qg, tag)) {
      u32 spins = SPIN_CAP;
      _Pragma("nounroll") do {
        RELOAD();
#pragma unroll
        for (int i = 0; i < 4; ++i) qg[i] = ldsc(rw, qo, (i >> 1) * 256 + (i & 1) * 16);
      } while (!tags_ok<4>(qg, tag) && --spins);
    }
    STAMP(2); PROBE_AT(2);
    u32x4 bq[8];
#pragma unroll
    for (int i = 0; i < 8; ++i) {
      const bool mine = ((lane >> 2) & 3) == (i >> 1);
      const int hf = i & 1;
      bq[i] = u32x4{mine ? qg[2 * hf][0] : 0u, mine ? qg[2 * hf][2] : 0u, mine ? qg[2 * hf + 1][0] : 0u, mine ? qg[2 * hf + 1][2] : 0u};
    }
    Soft st; st.m = -INFINITY; st.ml = -INFINITY; st.l = 0.f; st.acc = f32x4{0.f, 0.f, 0.f, 0.f};
    u32x4 bp0 = {0u, 0u, 0u, 0u}, bp1 = {0u, 0u, 0u, 0u};
    const u32 hm = odd ? 0xffff0000u : 0x0000ffffu;
    for (int it = 0; it < TPW / 2 - 1; ++it) {
      so += 65536;
      tile<true>(ka, va, bq, bp0, bp1, st, odd, hm, rk, rv, ko, vo, so);
      tile<true>(kb, vb, bq, bp0, bp1, st, odd, hm, rk, rv, ko, vo, so + 32768);
      STAMP(8 + it); if (it == 7) PROBE_AT(3);
    }
    tile<false>(ka, va, bq, bp0, bp1, st, odd, hm, rk, rv, ko, vo, so);
    tile<false>(kb, vb, bq, bp0, bp1, st, odd, hm, rk, rv, ko, vo, so);
    STAMP(3); PROBE_AT(4);
    float l = st.l;
    l += dppf<XOR1>(l); l += dppf<ROR4>(l); l += dppf<ROR8>(l); l = sum32(sum16(l));
    LAS float* lf = (LAS float*)lds;
#pragma unroll
    for (int i = 0; i < 4; ++i) lf[L_ACC + (wave * 4 + i) * 64 + lane] = st.acc[i];
    if (lane < 4) lf[L_ML + ((lane >> 1) * 2 + (lane & 1)) * 4 + wave] = odd ? l : st.ml;
    if (lane == 4) lds[L_TAG] = tag;
    if (wave == 0 && lane == 5) lds[L_TAG + 1] = probe;
    wg_barrier();
    STAMP(4);
    if (wave < 2) {
      const f32x4 mw = *(const LAS f32x4*)(lf + L_ML + wave * 8), lw = *(const LAS f32x4*)(lf + L_ML + wave * 8 + 4);
      const float M = fmaxf(fmaxf(mw[0], mw[1]), fmaxf(mw[2], mw[3]));
      float L = 0.f; f32x2 o = {0.f, 0.f};
      const int src = ((lane >> 2) & 3) * 64 + (lane >> 4) * 16 + (lane & 3) * 4 + 2 * wave;
#pragma unroll
      for (int w = 0; w < 4; ++w) {
        const float ew = ex2(mw[w] - M);
        L = fmaf(ew, lw[w], L);
        o += ew * *(const LAS f32x2*)(lf + L_ACC + w * 256 + src);
      }
      const float inv = 1.0f / L;
      const u32 rec = WS_AG + bid * AG_REC;
      stsc(rw, rec + wave * 1024 + lane * 16, u32x4{asu(o[0] * inv), tag, asu(o[1] * inv), tag});
      if (lane == 0) __builtin_amdgcn_raw_buffer_store_b64(u32x2{asu(M + __builtin_amdgcn_logf(L)), tag}, rw, rec + 2048 + wave * 8, 0, AUX_SC1);
    }
    STAMP(5);
    STAMP_OUT();
    return;
  }

  const int aw = wave - 4, g = lane >> 4, c = lane & 15;
  if (bid < Q0 + NQ) {
    const rsrc_t rx = mkrsrc(x, 2 * DM * 2), rq = mkrsrc(wq, DM * HQ * DH * 2);
    u32 xd[9]; u32x4 a[18];
#pragma unroll
    for (int k = 0; k < 9; ++k) xd[k] = __builtin_amdgcn_raw_buffer_load_b32(rx, ((lane >> 5) * 288 + (lane & 31)) * 4, k * 128, 0);
    const u32 e = __builtin_amdgcn_raw_buffer_load_b32(rw, WS_E, 0, AUX_SC1);
    const u32 vo = (144 * aw + 36 * g + (c >> 3)) * 8192 + bid * 128 + (c & 7) * 16;
#pragma unroll
    for (int k = 0; k < 18; ++k) a[k] = ldnt(rq, vo, k * 16384);
    SBAR();
    wg_barrier();
    const Sel sel = make_sel(lane);
    u32 xm[18];
    layernorm(xd, lds + L_XN + aw * 576, lane, aw, xm);
    STAMP(1);
    f32x4 d0 = {0.f, 0.f, 0.f, 0.f}, d1 = d0;
#pragma unroll
    for (int k = 0; k < 18; ++k) { d0 = mfma(a[k], build_b(xm[k], sel, 0), d0); d1 = mfma(a[k], build_b(xm[k], sel, 1), d1); }
    LAS float* red = (LAS float*)lds + L_RED;
#pragma unroll
    for (int i = 0; i < 4; ++i) red[(aw * 4 + i) * 64 + lane] = lane < 32 ? d0[i] : d1[i];
    lds_fence();
    STAMP(2);
    u32 ticket = 0;
    if (lane == 0) ticket = __hip_atomic_fetch_add(lds + L_CTR, 1u, __ATOMIC_RELAXED, __HIP_MEMORY_SCOPE_WORKGROUP);
    if (__builtin_amdgcn_readfirstlane(ticket) == 3u) {
      asm volatile("" ::: "memory");
      const u32 tag = next_tag(__builtin_amdgcn_readfirstlane(e));
      const int bt = lane >> 5, cp = lane & 31, cc = cp >> 2;
      const int src = (cc & 3) * 64 + (cc >> 2) * 16 + (cp & 3) * 4 + 2 * bt;
      f32x2 s = {0.f, 0.f};
#pragma unroll
      for (int w = 0; w < 4; ++w) s += *(const LAS f32x2*)(red + w * 256 + src) + *(const LAS f32x2*)(red + w * 256 + src + 32);
      __builtin_amdgcn_raw_buffer_store_b64(u32x2{pk(s[0], s[1]), tag}, rw, WS_QG + (bt * 2048 + bid * 32 + cp) * 8, 0, AUX_SC1);
    }
    STAMP(3);
    STAMP_OUT();
    return;
  }
  if (bid < O0 + NO) {
    const int uo = bid - O0;
    const rsrc_t ro = mkrsrc(wo, HQ * DH * DM * 2);
    const u32 vo = (64 * uo + 16 * aw + 4 * g) * 1152 + c * 16, vo4 = c < 8 ? vo : OOB;
    u32x4 a[20];
    wg_barrier();
#pragma unroll
    for (int k = 0; k < 20; ++k) {
      if (k >= NAUX) pace<NAUX>();
      a[k] = ldnt(ro, k % 5 == 4 ? vo4 : vo, (k / 5) * 1152 + (k % 5) * 256);
      SBAR();
    }
    const Sel sel = make_sel(lane);
    const int bt = (lane >> 1) & 1, sp = ((lane >> 2) & 3) * 2 + (lane & 1);
    const u32 rec = WS_AG + ((bt * 16 + (uo >> 2)) * 8 + sp) * AG_REC;
    const u32 go = rec + (uo & 3) * 512 + aw * 128 + g * 32, lo = rec + 2048 + ((uo >> 1) & 1) * 8;
    STAMP(1);
    wg_barrier();
    STAMP(2);
    const u32 tag = lds[L_TAG];
    u32x4 g0, g1; u32x2 gl;
    _Pragma("nounroll") for (u32 spins = 0; spins < SPIN_CAP; ++spins) {
      RELOAD();
      g0 = ldsc(rw, go, 0); g1 = ldsc(rw, go, 16); gl = __builtin_amdgcn_raw_buffer_load_b64(rw, lo, 0, AUX_SC1);
      const bool ok = (g0[1] == tag) & (g0[3] == tag) & (g1[1] == tag) & (g1[3] == tag) & (gl[1] == tag);
      if (__builtin_amdgcn_ballot_w64(!ok) == 0ull) break;
    }
    STAMP(3);
    const float lse = asf(gl[0]);
    float M = fmaxf(lse, dppf<XOR1>(lse)); M = fmaxf(M, dppf<ROR4>(M)); M = fmaxf(M, dppf<ROR8>(M));
    const float wt = ex2(lse - M);
    float o[5] = {wt * asf(g0[0]), wt * asf(g0[2]), wt * asf(g1[0]), wt * asf(g1[2]), wt};
#pragma unroll
    for (int i = 0; i < 5; ++i) { o[i] += dppf<XOR1>(o[i]); o[i] += dppf<ROR4>(o[i]); o[i] += dppf<ROR8>(o[i]); }
    const float inv = 1.0f / o[4];
    const u32 om[2] = {pk(o[0] * inv, o[1] * inv), pk(o[2] * inv, o[3] * inv)};
    f32x4 y[5];
#pragma unroll
    for (int cb = 0; cb < 5; ++cb) y[cb] = f32x4{0.f, 0.f, 0.f, 0.f};
#pragma unroll
    for (int s = 0; s < 4; ++s) {
      const u32x4 b = build_b(om[s >> 1], sel, s & 1);
#pragma unroll
      for (int cb = 0; cb < 5; ++cb) y[cb] = mfma(a[s * 5 + cb], b, y[cb]);
    }
    y_to_lds(y, (LAS float*)lds + L_Y, lane, aw);
    wg_barrier();
    STAMP(4);
    y_publish<NO>((LAS float*)lds + L_Y, rw, WS_YG, uo, aw * 64 + lane, tag);
    STAMP(5);
    STAMP_OUT();
    return;
  }
  if (bid < F0 + NF) {
    const int uf = bid - F0;
    const rsrc_t rx = mkrsrc(x, 2 * DM * 2), rd = mkrsrc(wd, FF * DM * 2);
    u32 xd[9];
#pragma unroll
    for (int k = 0; k < 9; ++k) xd[k] = __builtin_amdgcn_raw_buffer_load_b32(rx, ((lane >> 5) * 288 + (lane & 31)) * 4, k * 128, 0);
    const u32 e = __builtin_amdgcn_raw_buffer_load_b32(rw, WS_E, 0, AUX_SC1);
    wg_barrier();
    const Sel sel = make_sel(lane);
    u32 xm[18];
    layernorm(xd, lds + L_XN + aw * 576, lane, aw, xm);
    f32x4 y[5];
#pragma unroll
    for (int cb = 0; cb < 5; ++cb) y[cb] = f32x4{0.f, 0.f, 0.f, 0.f};
    for (int su = 0; su < FSUB; ++su) {
      const int u = uf * FSUB + su;
      const GAS u32x4* gp = (const GAS u32x4*)((const GAS char*)(c < 8 ? wg : wu) + (size_t)(144 * aw + 36 * g) * (FF * 2) + u * 128 + (c & 7) * 16);
      const u32 vo = (64 * u + 16 * aw + 4 * g) * 1152 + c * 16, vo4 = c < 8 ? vo : OOB;
      u32x4 ring[NAUX], a[20];
#pragma unroll
      for (int k = 0; k < NAUX; ++k) ring[k] = __builtin_nontemporal_load(gp + k * (FF * 2 / 16));
      SBAR();
      f32x4 d = {0.f, 0.f, 0.f, 0.f};
#pragma unroll
      for (int k = 0; k < 36; ++k) {
        asm volatile("" : "+v"(xm[k >> 1]));
        d = mfma(ring[k % NAUX], build_b(xm[k >> 1], sel, k & 1), d);
        SBAR();
        if (k + NAUX < 36) ring[k % NAUX] = __builtin_nontemporal_load(gp + (k + NAUX) * (FF * 2 / 16));
        SBAR();
      }
      LAS float* red = (LAS float*)lds + L_RED + su * 1024;
#pragma unroll
      for (int i = 0; i < 4; ++i) red[(aw * 4 + i) * 64 + lane] = d[i];
#pragma unroll
      for (int k = 0; k < 20; ++k) {
        if (k >= NAUX) pace<NAUX>();
        a[k] = ldnt(rd, k % 5 == 4 ? vo4 : vo, (k / 5) * 1152 + (k % 5) * 256);
        SBAR();
      }
      aux_sync(lds + L_CTR, 4 * (su + 1), lane);
      const int cc = 2 * aw + (g >> 1);
      const int src = ((lane >> 2) & 3) * 256 + (cc & 3) * 64 + ((lane & 1) * 2 + (cc >> 2)) * 16 + (g & 1) * 8 + (lane & 2);
      const f32x2 r0 = *(const LAS f32x2*)(red + src), r1 = *(const LAS f32x2*)(red + src + 4);
      float h[4] = {r0[0], r0[1], r1[0], r1[1]};
#pragma unroll
      for (int i = 0; i < 4; ++i) {
        h[i] += dppf<ROR4>(h[i]); h[i] += dppf<ROR8>(h[i]);
        const float own = rbf(h[i]), oth = dppf<XOR1>(own);
        const float gt = (lane & 1) ? oth : own, up = (lane & 1) ? own : oth;
        h[i] = rbf(silu(gt)) * up;
      }
      const u32 hm2[2] = {pk(h[0], h[1]), pk(h[2], h[3])};
#pragma unroll
      for (int s = 0; s < 4; ++s) {
        const u32x4 b = build_b(hm2[s >> 1], sel, s & 1);
#pragma unroll
        for (int cb = 0; cb < 5; ++cb) y[cb] = mfma(a[s * 5 + cb], b, y[cb]);
      }
      STAMP(1 + su);
    }
    y_to_lds(y, (LAS float*)lds + L_Y, lane, aw);
    aux_sync(lds + L_CTR, 4 * (FSUB + 1), lane);
    y_publish<NF>((LAS float*)lds + L_Y, rw, WS_FG, uf, aw * 64 + lane, next_tag(__builtin_amdgcn_readfirstlane(e)));
    STAMP(5);
    STAMP_OUT();
    return;
  }
  if (bid < Z0 + NZ && aw == 0) {
    const int f = bid - Z0, np = lane & 7;
    const u32 oi = (np & 1) * 288 + 4 * f + (np >> 1);
    u32 xv = ((const u32*)x)[oi];
    GAS u32* op = (GAS u32*)out + oi;
    wg_barrier();
    asm volatile("" : "+v"(xv), "+v"(op));
    wg_barrier();
    STAMP(1);
    const u32 tag = lds[L_TAG];
    u32x4 r[8];
    _Pragma("nounroll") for (u32 spins = 0; spins < SPIN_CAP; ++spins) {
      bool ok = true;
      RELOAD();
#pragma unroll
      for (int k = 0; k < 3; ++k) r[k] = ldsc(rw, WS_FG + f * (NF * 128) + lane * 16, k * 1024);
#pragma unroll
      for (int k = 0; k < 3; ++k) ok &= (r[k][1] == tag) & (r[k][3] == tag);
      if (__builtin_amdgcn_ballot_w64(!ok) == 0ull) break;
    }
    float y0 = (asf(r[0][0]) + asf(r[1][0])) + asf(r[2][0]), y1 = (asf(r[0][2]) + asf(r[1][2])) + asf(r[2][2]);
    y0 += dppf<ROR8>(y0); y1 += dppf<ROR8>(y1);
    y0 = sum32(sum16(y0)); y1 = sum32(sum16(y1));
    float g0 = rbf(silu(rbf(y0))), g1 = rbf(silu(rbf(y1)));
    asm volatile("" : "+v"(g0), "+v"(g1));
    STAMP(2);
    _Pragma("nounroll") for (u32 spins = 0; spins < SPIN_CAP; ++spins) {
      bool ok = true;
      RELOAD();
#pragma unroll
      for (int k = 0; k < 8; ++k) r[k] = ldsc(rw, WS_YG + f * (NO * 128) + lane * 16, k * 1024);
#pragma unroll
      for (int k = 0; k < 8; ++k) ok &= (r[k][1] == tag) & (r[k][3] == tag);
      if (__builtin_amdgcn_ballot_w64(!ok) == 0ull) break;
    }
    STAMP(3);
    float s0 = 0.f, s1 = 0.f;
#pragma unroll
    for (int k = 0; k < 8; ++k) { s0 += asf(r[k][0]); s1 += asf(r[k][2]); }
    s0 += dppf<ROR8>(s0); s1 += dppf<ROR8>(s1);
    s0 = sum32(sum16(s0)); s1 = sum32(sum16(s1));
    if (lane < 8) *op = pk(lo16(xv) + rbf(rbf(s0) * g0), hi16(xv) + rbf(rbf(s1) * g1));
    if (f == 0 && lane == 0) __builtin_amdgcn_raw_buffer_store_b32(tag, rw, WS_E, 0, AUX_SC1);
    if (f == 0) {
      probe = __builtin_amdgcn_readfirstlane(lds[L_TAG + 1]);
      PROBE_AT(5);
      while ((u32)__builtin_amdgcn_s_memrealtime() - tstart < 20000u + 10u * probe) __builtin_amdgcn_s_sleep(1);
    }
    STAMP(4);
    STAMP_OUT();
    return;
  }
}

extern "C" void kernel_launch(void* const* d_in, const int* in_sizes, int n_in, void* d_out, int out_size, void* d_ws, size_t ws_size,
                              hipStream_t stream) {
  fused_decode<<<NWG, NTHR, 0, stream>>>((const u16*)d_in[1], (const u16*)d_in[2], (unsigned char*)d_ws, (const u16*)d_in[0], (const u16*)d_in[3],
                                          (const u16*)d_in[4], (const u16*)d_in[6], (const u16*)d_in[7], (const u16*)d_in[8], (u16*)d_out);
}
```

```cpp
#include <hip/hip_runtime.h>
#include <hip/hip_bf16.h>
#include <stdint.h>

typedef unsigned u32;
typedef unsigned short u16;
typedef u32 u32x2 __attribute__((ext_vector_type(2)));
typedef u32 u32x3 __attribute__((ext_vector_type(3)));
typedef u32 u32x4 __attribute__((ext_vector_type(4)));
typedef float f32x2 __attribute__((ext_vector_type(2)));
typedef float f32x4 __attribute__((ext_vector_type(4)));
typedef __bf16 bf16x8 __attribute__((ext_vector_type(8)));
typedef __bf16 bf16x2 __attribute__((ext_vector_type(2)));
typedef __amdgpu_buffer_rsrc_t rsrc_t;
#define GAS __attribute__((address_space(1)))
#define LAS __attribute__((address_space(3)))

#define NPRO 5
#define NFL 5
#define NHI 9
#define SCH_DT 220
#define SCH_SH 5
#define SCH_BIAS 32
#define SCH_IT0 2
#define SCH_TAB 0x0001114955FFull
#define LEAD_O 0
#define LEAD_F 0
#define QPOS 26
#define QFIRST 8
#define NAUX 4
#define NE0 3
#define NLATE 8
#define SPRINT 0
#define DLY_O 0
#define DLY_ZY 64
#define DLY_ZF 40
#define CODEPF_BYTES 23040
#define CODEPF 1

constexpr int DM = 576, HQ = 32, SK = 32768, DH = 128, FF = 4608;
constexpr int NWG = 256, NTHR = 512;
constexpr u32 CHUNK = 4096u * DH * 2u;
constexpr int TPW = 32;
constexpr int AUX_NT = 2, AUX_SC1 = 16;
constexpr u32 OOB = 0x80000000u;
constexpr int Q0 = 0, NQ = 64, O0 = 64, NO = 64, F0 = 128, NF = 72, Z0 = 200;
constexpr u32 WS_E = 0, WS_QG = 4096, WS_AG = WS_QG + 32768, WS_YG = WS_AG + NWG * 1024, WS_FG = WS_YG + 36 * NF * 128,
              WS_SC = WS_FG + 36 * NF * 128, SC_STR = 4352, WS_END = WS_SC + 16 * 8 * SC_STR;
constexpr u32 SPIN_CAP = 400000;
constexpr int L_CTR = 0, L_TAG = 4, L_ML = 8, L_ACC = 32, ACC_W = 352, L_XN = L_ACC + 4 * ACC_W, L_RED = L_XN + 4 * 576, L_Y = L_RED + 1024,
              L_S = L_Y + 4 * 1600, L_END = L_S + 4 * 2048;
constexpr float LOG2E = 1.4426950408889634f;

#define STAMPZ(k)
#define STAMPZ_OUT()
#define STAMP(k)
#define STAMP_OUT()
#define STAMP2(k)

__device__ __forceinline__ rsrc_t mkrsrc(const void* p, u32 bytes) { return __builtin_amdgcn_make_buffer_rsrc((void*)p, 0, bytes, 0x00020000); }
__device__ __forceinline__ u32x4 ldnt(rsrc_t r, u32 vo, u32 so) { return __builtin_amdgcn_raw_buffer_load_b128(r, vo, so, AUX_NT); }
__device__ __forceinline__ u32x4 ldsc(rsrc_t r, u32 vo, u32 so) { return __builtin_amdgcn_raw_buffer_load_b128(r, vo, so, AUX_SC1); }
__device__ __forceinline__ void stsc(rsrc_t r, u32 vo, u32x4 v) { __builtin_amdgcn_raw_buffer_store_b128(v, r, vo, 0, AUX_SC1); }
__device__ __forceinline__ f32x4 mfma(u32x4 a, u32x4 b, f32x4 c) {
  return __builtin_amdgcn_mfma_f32_16x16x32_bf16(__builtin_bit_cast(bf16x8, a), __builtin_bit_cast(bf16x8, b), c, 0, 0, 0);
}
__device__ __forceinline__ float asf(u32 x) { return __builtin_bit_cast(float, x); }
__device__ __forceinline__ u32 asu(float x) { return __builtin_bit_cast(u32, x); }
__device__ __forceinline__ u32 pk(float lo, float hi) { bf16x2 v = {(__bf16)lo, (__bf16)hi}; return __builtin_bit_cast(u32, v); }
__device__ __forceinline__ float rbf(float x) { return (float)(__bf16)x; }
__device__ __forceinline__ float lo16(u32 x) { return asf(x << 16); }
__device__ __forceinline__ float hi16(u32 x) { return asf(x & 0xffff0000u); }
template <int CTRL> __device__ __forceinline__ u32 dppu(u32 x) { return (u32)__builtin_amdgcn_mov_dpp((int)x, CTRL, 0xf, 0xf, true); }
template <int CTRL> __device__ __forceinline__ float dppf(float x) { return asf(dppu<CTRL>(asu(x))); }
constexpr int XOR1 = 0xB1, XOR2 = 0x4E, XOR7 = 0x141, ROR4 = 0x124, ROR8 = 0x128;
__device__ __forceinline__ float sum16(float x) { auto s = __builtin_amdgcn_permlane16_swap(asu(x), asu(x), false, false); return asf(s[0]) + asf(s[1]); }
__device__ __forceinline__ float sum32(float x) { auto s = __builtin_amdgcn_permlane32_swap(asu(x), asu(x), false, false); return asf(s[0]) + asf(s[1]); }
__device__ __forceinline__ float max16(float x) { auto s = __builtin_amdgcn_permlane16_swap(asu(x), asu(x), false, false); return fmaxf(asf(s[0]), asf(s[1])); }
__device__ __forceinline__ float max32(float x) { auto s = __builtin_amdgcn_permlane32_swap(asu(x), asu(x), false, false); return fmaxf(asf(s[0]), asf(s[1])); }
__device__ __forceinline__ float ex2(float x) { return __builtin_amdgcn_exp2f(x); }
__device__ __forceinline__ float rcp(float x) { return __builtin_amdgcn_rcpf(x); }
__device__ __forceinline__ float silu(float x) { return x * rcp(1.0f + ex2(-LOG2E * x)); }
__device__ __forceinline__ u32 now32() { return (u32)__builtin_amdgcn_s_memrealtime(); }
template <int N> __device__ __forceinline__ void pace() { asm volatile("s_waitcnt vmcnt(%0)" ::"n"(N - 1) : "memory"); }
template <int BIT, int EXTRA> __device__ __forceinline__ void pacev(u32 mask) {
  asm volatile("s_waitcnt vmcnt(%1)\n\ts_bitcmp1_b32 %0, %3\n\ts_cbranch_scc0 1f\n\ts_waitcnt vmcnt(%2)\n1:" ::"s"(mask), "n"(NHI - 1 + EXTRA), "n"(NFL - 1 + EXTRA), "n"(BIT) : "memory", "scc");
}
__device__ __forceinline__ u32 pace_mask(u32 now, u32 due) {
  int lev = (int)(now - due) >> SCH_SH;
  asm("s_max_i32 %0, %0, 0\n\ts_min_i32 %0, %0, 7" : "+s"(lev) : : "scc");
  return (u32)(SCH_TAB >> (8 * lev));
}
#define SBAR() __builtin_amdgcn_sched_barrier(0)
#define RELOAD() asm volatile("" ::: "memory")
__device__ __forceinline__ void lds_fence() { asm volatile("s_waitcnt lgkmcnt(0)" ::: "memory"); }
__device__ __forceinline__ void wg_barrier() { lds_fence(); __builtin_amdgcn_s_barrier(); asm volatile("" ::: "memory"); }
template <int N> __device__ __forceinline__ bool tags_ok(const u32x4* r, u32 tag) {
  bool bad = false;
#pragma unroll
  for (int i = 0; i < N; ++i) bad |= (r[i][1] != tag) | (r[i][3] != tag);
  return __builtin_amdgcn_ballot_w64(bad) == 0ull;
}
__device__ __forceinline__ u32 next_tag(u32 e) { return e * 1664525u + 1013904223u; }

struct Sched { u32 mask, due, tag, lead, ldo, sto; bool sample; u32x2 row; };

template <int W, int T> __device__ __forceinline__ u32 bank_mov(u32 old, u32 z) {
  constexpr int ctrl = W == T ? 0xE4 : (W > T ? 0x110 + 4 * (W - T) : 0x100 + 4 * (T - W));
  return (u32)__builtin_amdgcn_update_dpp((int)old, (int)z, ctrl, 0xf, 1 << W, false);
}
template <int T> __device__ __forceinline__ void build_p(u32x4& b, u32 z) {
  b[0] = bank_mov<0, T>(b[0], z); b[1] = bank_mov<1, T>(b[1], z); b[2] = bank_mov<2, T>(b[2], z); b[3] = bank_mov<3, T>(b[3], z);
}

template <int GRP> __device__ __forceinline__ void sched_step(Sched& sc, u32 now, rsrc_t rw, int it) {
  if (GRP == 0) {
    sc.row = __builtin_amdgcn_raw_buffer_load_b64(rw, sc.ldo, it * (8 * SC_STR), AUX_SC1);
    if (sc.sample) __builtin_amdgcn_raw_buffer_store_b64(u32x2{now + sc.lead, sc.tag}, rw, sc.sto, (it + 1) * (8 * SC_STR), AUX_SC1);
  } else {
    const bool ok = __builtin_amdgcn_ballot_w64(sc.row[1] == sc.tag) == 0xffull;
    u32 d = sc.row[0] - now;
    d += dppu<XOR1>(d); d += dppu<XOR2>(d); d += dppu<XOR7>(d);
    const u32 due = now + ((int)__builtin_amdgcn_readfirstlane(d) >> 3) + (3 * SCH_DT - SCH_BIAS) - sc.lead;
    if (ok && it >= SCH_IT0) sc.due = due;
  }
  sc.mask = pace_mask(now, sc.due);
  if (SPRINT && GRP == 2 && it == 14) sc.mask = 0u;
  sc.due += SCH_DT;
}
template <int GRP, int I>
__device__ __forceinline__ void refill(u32x4& slot, rsrc_t r, u32 vo, u32 so, Sched& sc, u32& now, rsrc_t rw, int it) {
  pacev<I, (GRP == 0 && I >= 2 && I < 2 + NFL) ? 1 : 0>(sc.mask);
  slot = ldnt(r, vo, so + GRP * 32768 + I * 1024);
  if (!(GRP & 1) && I == 0) now = now32();
  if (!(GRP & 1) && I == 1) sched_step<GRP>(sc, now, rw, it);
  SBAR();
}
template <int T> __device__ __forceinline__ void k_pick(u32x4& u, f32x4 d) {
  u[0] = bank_mov<T, 0>(u[0], asu(d[0])); u[1] = bank_mov<T, 1>(u[1], asu(d[1])); u[2] = bank_mov<T, 2>(u[2], asu(d[2])); u[3] = bank_mov<T, 3>(u[3], asu(d[3]));
}
template <int GRP>
__device__ __forceinline__ float k_tile(u32x4 (&k)[8], u32x4 bq0, u32x4 bq1, u32x4& u, rsrc_t r, u32 vo, u32 so, Sched& sc, rsrc_t rw, int it) {
  const f32x4 z = {0.f, 0.f, 0.f, 0.f};
  f32x4 da, db;
  u32 now = 0;
#define RF(I) SBAR(); refill<GRP, I>(k[I], r, vo, so, sc, now, rw, it);
  da = mfma(k[0], bq0, z);                     RF(0)
  da = mfma(k[1], bq1, da);                    RF(1)
  db = mfma(k[2], bq0, z);                     RF(2)
  db = mfma(k[3], bq1, db); k_pick<0>(u, da);  RF(3)
  da = mfma(k[4], bq0, z);                     RF(4)
  da = mfma(k[5], bq1, da); k_pick<1>(u, db);  RF(5)
  db = mfma(k[6], bq0, z);                     RF(6)
  db = mfma(k[7], bq1, db); k_pick<2>(u, da);  RF(7)
#undef RF
  k_pick<3>(u, db);
  const float s = (asf(u[0]) + asf(u[1])) + (asf(u[2]) + asf(u[3]));
  return rbf(rbf(s) * 0.08838834764831845f);
}
__device__ __forceinline__ u32x2 probs(float s, float ml, float& l, u32 hm) {
  const float p = ex2(fmaf(s, LOG2E, -ml));
  l += p;
  const u32 t = pk(p, p);
  return u32x2{dppu<0xA0>(t) & hm, dppu<0xF5>(t) & hm};
}
template <int GRP>
__device__ __forceinline__ void v_tile(u32x4 (&v)[8], u32x2 z, f32x4& acc, u32x4& bp0, u32x4& bp1, rsrc_t r, u32 vo, u32 so, Sched& sc, rsrc_t rw, int it) {
  u32 now = 0;
#define PV_STEP(U, BP) build_p<(U) / 2>(BP, z[(U) & 1]); acc = mfma(v[U], BP, acc); SBAR(); refill<GRP, U>(v[U], r, vo, so, sc, now, rw, it);
  PV_STEP(0, bp0) PV_STEP(1, bp1) PV_STEP(2, bp0) PV_STEP(3, bp1) PV_STEP(4, bp0) PV_STEP(5, bp1) PV_STEP(6, bp0) PV_STEP(7, bp1)
#undef PV_STEP
}
template <int K> __device__ __forceinline__ u32x4 row_load(rsrc_t r, u32 vo, u32 vo4) { return ldnt(r, K % 5 == 4 ? vo4 : vo, (K / 5) * 1152 + (K % 5) * 256); }
template <int FROM, int CNT, int K0>
__device__ __forceinline__ void v_last(const u32x4 (&v)[8], u32x2 z, f32x4& acc, u32x4& bp0, u32x4& bp1, u32x4 (&a)[20], rsrc_t r, u32 vo, u32 vo4) {
#define PV_STEP(U, BP) build_p<(U) / 2>(BP, z[(U) & 1]); acc = mfma(v[U], BP, acc); SBAR(); \
  if constexpr (U >= FROM && U < FROM + CNT) { a[K0 + U - FROM] = row_load<K0 + U - FROM>(r, vo, vo4); SBAR(); }
  PV_STEP(0, bp0) PV_STEP(1, bp1) PV_STEP(2, bp0) PV_STEP(3, bp1) PV_STEP(4, bp0) PV_STEP(5, bp1) PV_STEP(6, bp0) PV_STEP(7, bp1)
#undef PV_STEP
}
template <int LO, int HI> __device__ __forceinline__ void rows_load(u32x4 (&a)[20], rsrc_t r, u32 vo, u32 vo4) {
  if constexpr (LO < HI) { pace<NLATE>(); a[LO] = row_load<LO>(r, vo, vo4); SBAR(); rows_load<LO + 1, HI>(a, r, vo, vo4); }
}

struct Sel { u32 s[4][2]; };
__device__ __forceinline__ Sel make_sel(int lane) {
  Sel r; const int wp = (lane >> 2) & 3; const bool hb = lane & 1;
#pragma unroll
  for (int par = 0; par < 2; ++par) {
    const u32 two = ((2 * par + 1) << 8) | (2 * par);
    const u32 base = hb ? ((two << 16) | 0x0c0cu) : (0x0c0c0000u | two);
#pragma unroll
    for (int w = 0; w < 4; ++w) r.s[w][par] = wp == w ? base : 0x0c0c0c0cu;
  }
  return r;
}
__device__ __forceinline__ void pin_sel(Sel& s) {
  asm volatile("" : "+v"(s.s[0][0]), "+v"(s.s[0][1]), "+v"(s.s[1][0]), "+v"(s.s[1][1]), "+v"(s.s[2][0]), "+v"(s.s[2][1]), "+v"(s.s[3][0]), "+v"(s.s[3][1]));
}
__device__ __forceinline__ u32x4 build_b(u32 d, const Sel& sel, int par) {
  u32x4 b;
#pragma unroll
  for (int w = 0; w < 4; ++w) b[w] = __builtin_amdgcn_perm(d, d, sel.s[w][par]);
  return b;
}
__device__ __forceinline__ void load_x(rsrc_t rx, u32 xo, u32 (&xd)[9]) {
#pragma unroll
  for (int k = 0; k < 3; ++k) {
    const u32x3 t = __builtin_amdgcn_raw_buffer_load_b96(rx, xo, k * 384, 0);
    xd[3 * k] = t[0]; xd[3 * k + 1] = t[1]; xd[3 * k + 2] = t[2];
  }
}
__device__ __forceinline__ void layernorm(const u32 (&xd)[9], LAS u32* xn, int lane, int aw, u32 (&xm)[18]) {
  f32x2 v[9], s2 = {0.f, 0.f};
#pragma unroll
  for (int k = 0; k < 9; ++k) { v[k] = f32x2{lo16(xd[k]), hi16(xd[k])}; s2 += v[k]; }
  float s = s2[0] + s2[1];
  s += dppf<XOR1>(s); s += dppf<XOR2>(s); s += dppf<ROR4>(s); s += dppf<ROR8>(s); s = sum16(s);
  const float mean = s * (1.0f / DM);
  f32x2 q2 = {0.f, 0.f};
#pragma unroll
  for (int k = 0; k < 9; ++k) { const f32x2 a = v[k] - mean; q2 += a * a; }
  float q = q2[0] + q2[1];
  q += dppf<XOR1>(q); q += dppf<XOR2>(q); q += dppf<ROR4>(q); q += dppf<ROR8>(q); q = sum16(q);
  const float mb = rbf(mean), den = rbf(__builtin_amdgcn_sqrtf(rbf(rbf(q * (1.0f / DM)) + 1e-5f))), inv = rcp(den);
#pragma unroll
  for (int k = 0; k < 9; ++k) {
    const f32x2 a = v[k] - mb;
    const u32 r = pk(a[0], a[1]);
    const f32x2 b = f32x2{lo16(r), hi16(r)} * inv;
    xn[(lane >> 5) * 288 + (lane & 31) * 3 + 96 * (k / 3) + k % 3] = pk(b[0], b[1]);
  }
  lds_fence();
  const LAS u32x2* src = (const LAS u32x2*)(xn + ((lane >> 1) & 1) * 288 + 72 * aw + 18 * (lane >> 4));
#pragma unroll
  for (int k = 0; k < 9; ++k) { const u32x2 t = src[k]; xm[2 * k] = t[0]; xm[2 * k + 1] = t[1]; }
}
__device__ __forceinline__ u32x4 pack4(f32x4 v, u32 tag) {
  return u32x4{__builtin_amdgcn_perm(asu(v[1]), asu(v[0]), 0x05030201u), __builtin_amdgcn_perm(tag, asu(v[1]), 0x05040302u),
               __builtin_amdgcn_perm(asu(v[3]), asu(v[2]), 0x05030201u), __builtin_amdgcn_perm(tag, asu(v[3]), 0x05040302u)};
}
__device__ __forceinline__ f32x4 unpack4(u32x4 r) {
  return f32x4{asf(r[0] << 8), asf(__builtin_amdgcn_perm(r[1], r[0], 0x0504030cu)), asf(r[2] << 8), asf(__builtin_amdgcn_perm(r[3], r[2], 0x0504030cu))};
}
template <int N> __device__ __forceinline__ bool tags16_ok(const u32x4* r, u32 tag) {
  u32 bad = 0;
#pragma unroll
  for (int i = 0; i < N; ++i) bad |= (r[i][1] ^ (tag << 16)) | (r[i][3] ^ (tag << 16));
  return __builtin_amdgcn_ballot_w64(bad > 0xffffu) == 0ull;
}
template <int N> __device__ __forceinline__ float sum_parts(const u32x4* r) {
  f32x4 a = unpack4(r[0]);
#pragma unroll
  for (int i = 1; i < N; ++i) a += unpack4(r[i]);
  const auto s0 = __builtin_amdgcn_permlane32_swap(asu(a[0]), asu(a[2]), false, false), s1 = __builtin_amdgcn_permlane32_swap(asu(a[1]), asu(a[3]), false, false);
  const auto s2 = __builtin_amdgcn_permlane16_swap(asu(asf(s0[0]) + asf(s0[1])), asu(asf(s1[0]) + asf(s1[1])), false, false);
  float t = asf(s2[0]) + asf(s2[1]);
  t += dppf<ROR4>(t); t += dppf<ROR8>(t);
  return t;
}

typedef u32 u32x16 __attribute__((ext_vector_type(16)));
__device__ __forceinline__ const char* mkptr(u32 lo, u32 hi) { return (const char*)(GAS char*)(((uint64_t)hi << 32) | lo); }

__global__ void __launch_bounds__(NTHR, 2)
fused_decode(const u16* __restrict__, const u16* __restrict__, unsigned char*, const u16* __restrict__, const u16* __restrict__,
             const u16* __restrict__, const u16* __restrict__, const u16* __restrict__, const u16* __restrict__, u16*) {
  __shared__ __attribute__((aligned(16))) u32 smem[L_END];
  LAS u32* lds = (LAS u32*)smem;
  const int lane = threadIdx.x & 63, wave = __builtin_amdgcn_readfirstlane(threadIdx.x >> 6), bid = blockIdx.x;
  {
    u32 pf; uint64_t pc;
    asm volatile("s_getpc_b64 %1\n\ts_cmp_lt_u32 %3, 4\n\ts_cbranch_scc0 1f\n\tglobal_load_dword %0, %2, %1\n\ts_waitcnt vmcnt(0)\n1:"
                 : "=&v"(pf), "=&s"(pc) : "v"(min((threadIdx.x & 255u) * 128u, (u32)CODEPF_BYTES)), "s"((u32)(wave - 4) | ((u32)((bid >> 4) - (O0 >> 4)) << 4)) : "scc");
  }
  const int g = lane >> 4;
  STAMPZ(0);
#define ARGS() \
  u32x16 ar; u32x4 ar2; \
  asm volatile("s_load_dwordx16 %0, %2, 0x0\n\ts_load_dwordx4 %1, %2, 0x40\n\ts_waitcnt lgkmcnt(0)" : "=&s"(ar), "=&s"(ar2) : "s"(__builtin_amdgcn_kernarg_segment_ptr())); \
  const char *kc = mkptr(ar[0], ar[1]), *vc = mkptr(ar[2], ar[3]), *x = mkptr(ar[6], ar[7]), *wq = mkptr(ar[8], ar[9]), *wo = mkptr(ar[10], ar[11]), \
             *wg = mkptr(ar[12], ar[13]), *wu = mkptr(ar[14], ar[15]), *wd = mkptr(ar2[0], ar2[1]); \
  char *ws = (char*)mkptr(ar[4], ar[5]), *out = (char*)mkptr(ar2[2], ar2[3]); \
  const rsrc_t rw = mkrsrc(ws, WS_END);

  if (__builtin_expect(wave < 4, 1)) {
    ARGS()
    const u32 ko = ((lane >> 2) & 3) * 256 + (4 * g + (lane & 3)) * 16;
    const rsrc_t rk = mkrsrc((const char*)kc + (size_t)bid * CHUNK, CHUNK), rv = mkrsrc((const char*)vc + (size_t)bid * CHUNK, CHUNK);
    const bool odd = lane & 1;
    const u32 vo = lane * 16;
    u32x4 r0[8], r1[8], r2[8], r3[8], qg;
    u32 so = wave * 8192, e = 0, qo = 0;
    if (bid < Q0 + NQ) wg_barrier();
#pragma unroll
    for (int p = 0; p < 32; ++p) {
      if (p == NPRO) STAMP2(24);
      if (p == NPRO) {
        u32 ln = lane; asm volatile("" : "+v"(ln));
        qo = WS_QG + (bid >> 3) * 1024 + ((ln >> 1) & 1) * 512 + (ln >> 2) * 32 + (ln & 1) * 16;
      }
      if (p == QPOS) {
        e = __builtin_amdgcn_raw_buffer_load_b32(rw, WS_E, 0, AUX_SC1);
        qg = ldsc(rw, qo, 0);
      }
      if (p >= QPOS && p < QPOS + NPRO) pace<NPRO + 2>();
      else if (p >= NPRO) pace<NPRO>();
      if (p == NPRO) STAMP2(26);
      if (p == 8 + NPRO) STAMP2(28);
      if (p == 16 + NPRO) STAMP2(29);
      const u32x4 t = ldnt(rk, ko, so + (p >> 3) * 32768 + (p & 7) * 1024);
      if (p < 8) r0[p] = t; else if (p < 16) r1[p - 8] = t; else if (p < 24) r2[p - 16] = t; else r3[p - 24] = t;
      SBAR();
    }
    STAMP(1);
    const u32 tag = next_tag(__builtin_amdgcn_readfirstlane(e));
    if (!tags_ok<1>(&qg, tag)) {
      u32 spins = SPIN_CAP;
      _Pragma("nounroll") do { RELOAD(); qg = ldsc(rw, qo, 0); } while (!tags_ok<1>(&qg, tag) && --spins);
    }
    STAMP(2);
    Sched sc;
    sc.mask = 0xffu; sc.due = now32() + 0x20000000u; sc.tag = tag;
    sc.lead = (bid >= O0 && bid < F0) ? LEAD_O : ((bid >= F0 && bid < Z0) ? LEAD_F : 0);
    sc.ldo = lane < 8 ? WS_SC + lane * SC_STR : OOB;
    sc.sample = bid == 33 * (bid & 7) && wave == 0;
    sc.sto = lane == 0 ? WS_SC + (bid & 7) * SC_STR : OOB;
    LAS f32x4* sl = (LAS f32x4*)(lds + L_S) + wave * 512 + lane;
    float ml;
    {
      const u32 p0 = dppu<XOR1>(qg[0]), p1 = dppu<XOR1>(qg[2]);
      const u32x4 bq0 = {odd ? 0u : qg[0], odd ? 0u : qg[2], odd ? 0u : p0, odd ? 0u : p1}, bq1 = {odd ? p0 : 0u, odd ? p1 : 0u, odd ? qg[0] : 0u, odd ? qg[2] : 0u};
      u32x4 u = {0u, 0u, 0u, 0u};
      float mx = -INFINITY;
      for (int it = 0; it < 8; ++it) {
        int itv = it; asm volatile("" : "+s"(itv));
        const bool last = itv == 7;
        const rsrc_t r = mkrsrc((last ? vc : kc) + (size_t)bid * CHUNK, CHUNK);
        const u32 lo = last ? vo : ko;
        so = last ? wave * 8192 : so + 131072;
        f32x4 s4;
        s4[0] = k_tile<0>(r0, bq0, bq1, u, r, lo, so, sc, rw, it);
        s4[1] = k_tile<1>(r1, bq0, bq1, u, r, lo, so, sc, rw, it);
        s4[2] = k_tile<2>(r2, bq0, bq1, u, r, lo, so, sc, rw, it);
        s4[3] = k_tile<3>(r3, bq0, bq1, u, r, lo, so, sc, rw, it);
        sl[it * 64] = s4;
        mx = fmaxf(fmaxf(mx, fmaxf(s4[0], s4[1])), fmaxf(s4[2], s4[3]));
        STAMP(8 + it);
      }
      mx = fmaxf(mx, dppf<XOR1>(mx)); mx = fmaxf(mx, dppf<ROR4>(mx)); mx = fmaxf(mx, dppf<ROR8>(mx));
      ml = max32(max16(mx)) * LOG2E;
    }
    float l = 0.f;
    f32x4 acc = {0.f, 0.f, 0.f, 0.f};
    u32x4 bp0 = {0u, 0u, 0u, 0u}, bp1 = {0u, 0u, 0u, 0u};
    const u32 hm = odd ? 0xffff0000u : 0x0000ffffu;
    f32x4 s4 = sl[0];
    for (int it = 8; it < 15; ++it) {
      so += 131072;
      const f32x4 sn = sl[(it - 7) * 64];
      v_tile<0>(r0, probs(s4[0], ml, l, hm), acc, bp0, bp1, rv, vo, so, sc, rw, it);
      v_tile<1>(r1, probs(s4[1], ml, l, hm), acc, bp0, bp1, rv, vo, so, sc, rw, it);
      v_tile<2>(r2, probs(s4[2], ml, l, hm), acc, bp0, bp1, rv, vo, so, sc, rw, it);
      v_tile<3>(r3, probs(s4[3], ml, l, hm), acc, bp0, bp1, rv, vo, so, sc, rw, it);
      s4 = sn;
      STAMP(8 + it);
    }
    const bool isO = bid >= O0 && bid < F0, isF = bid >= F0 && bid < Z0;
    const int u = bid - (isF ? F0 : O0), c = lane & 15;
    const rsrc_t rl = mkrsrc(isF ? wd : wo, isF ? FF * DM * 2 : (isO ? HQ * DH * DM * 2 : 0));
    const u32 wv = (64 * u + 16 * wave + 4 * g) * 1152 + c * 16, wv4 = c < 8 ? wv : OOB;
    u32x4 a[20];
    v_last<0, NE0, 0>(r0, probs(s4[0], ml, l, hm), acc, bp0, bp1, a, rl, wv, wv4);
    v_last<0, 0, 0>(r1, probs(s4[1], ml, l, hm), acc, bp0, bp1, a, rl, wv, wv4);
    v_last<0, 0, 0>(r2, probs(s4[2], ml, l, hm), acc, bp0, bp1, a, rl, wv, wv4);
    const u32x2 z3 = probs(s4[3], ml, l, hm);
    l += dppf<XOR1>(l); l += dppf<ROR4>(l); l += dppf<ROR8>(l); l = sum32(sum16(l));
    LAS float* lf = (LAS float*)lds;
    if (lane < 4) lf[L_ML + lane * 4 + wave] = odd ? l : ml;
    lds[L_TAG] = tag;
    u32 ad = (L_ACC + wave * ACC_W + ((lane >> 1) & 1) * 176 + g * 40 + ((lane >> 2) & 3) * 2 + (lane & 1)) * 4;
    u32 as = (L_ACC + (lane >> 5) * 176 + ((lane >> 3) & 3) * 40 + (lane & 7) * 4) * 4, am = (L_ML + (lane >> 5) * 8) * 4;
    u32 ao = WS_AG + (((bid >> 3) & 15) * 16 + (lane >> 2)) * 1024 + (((bid >> 1) & 3) * 4 + (bid >> 7) * 2 + (bid & 1)) * 64 + (lane & 3) * 16;
    asm volatile("" : "+v"(ao), "+v"(ad), "+v"(as), "+v"(am));
    SBAR();
    v_last<3, 5, NE0>(r3, z3, acc, bp0, bp1, a, rl, wv, wv4);
    STAMP2(3);
#pragma unroll
    for (int i = 0; i < 4; ++i) *(LAS float*)((LAS char*)lds + ad + 32 * i) = acc[i];
    wg_barrier();
    STAMP(4);
    if (wave == 0) {
      const f32x4 mw = *(const LAS f32x4*)((LAS char*)lds + am), lw = *(const LAS f32x4*)((LAS char*)lds + am + 16);
      f32x4 pa[4];
#pragma unroll
      for (int w = 0; w < 4; ++w) pa[w] = *(const LAS f32x4*)((LAS char*)lds + as + w * (ACC_W * 4));
      SBAR();
      const float M = fmaxf(fmaxf(mw[0], mw[1]), fmaxf(mw[2], mw[3]));
      const f32x4 ew = {ex2(mw[0] - M), ex2(mw[1] - M), ex2(mw[2] - M), ex2(mw[3] - M)};
      const float L = fmaf(ew[0], lw[0], ew[1] * lw[1]) + fmaf(ew[2], lw[2], ew[3] * lw[3]);
      const f32x4 o = (ew[0] * pa[0] + ew[1] * pa[1] + ew[2] * pa[2] + ew[3] * pa[3]) * rcp(L);
      const u32 lse = asu(M + __builtin_amdgcn_logf(L));
      stsc(rw, ao, u32x4{pk(o[0], o[1]), __builtin_amdgcn_perm(tag, lse, 0x05040100u), pk(o[2], o[3]), __builtin_amdgcn_perm(tag, lse, 0x05040302u)});
    }
    STAMP2(5);
    if (isO || isF) {
      rows_load<NE0 + 5, 20>(a, rl, wv, wv4);
      Sel sel = make_sel(lane);
      u32 po = (isF ? WS_FG : WS_YG) + ((wave * 8 + (lane >> 3)) * NF + u) * 128 + (lane & 7) * 16, po2 = (wave == 0 && lane < 32) ? po + 32 * NF * 128 : OOB;
      u32 yd = L_Y * 4 + (wave * 400 + g * 20 + c) * 16, ys = L_Y * 4 + (wave * 80 + (lane & 3) * 20 + (lane >> 2)) * 16;
      pin_sel(sel);
      asm volatile("" : "+v"(po), "+v"(po2), "+v"(yd), "+v"(ys));
      STAMP(6);
      u32 m0, m1;
      if (isO) {
        const u32 go = WS_AG + (u * 4 + wave) * 1024 + c * 64 + g * 16;
        if (DLY_O) __builtin_amdgcn_s_sleep(DLY_O);
        u32x4 r;
        _Pragma("nounroll") for (u32 spins = SPIN_CAP; spins; --spins) {
          RELOAD();
          r = ldsc(rw, go, 0);
          if (tags16_ok<1>(&r, tag)) break;
        }
        STAMP2(7);
        const float lse = asf(__builtin_amdgcn_perm(r[3], r[1], 0x05040100u));
        float M = fmaxf(lse, dppf<XOR1>(lse)); M = fmaxf(M, dppf<ROR4>(M)); M = fmaxf(M, dppf<ROR8>(M));
        const float wt = ex2(lse - M);
        float o[5] = {wt * lo16(r[0]), wt * hi16(r[0]), wt * lo16(r[2]), wt * hi16(r[2]), wt};
#define RED5(C) _Pragma("unroll") for (int i = 0; i < 5; ++i) { o[i] += dppf<C>(o[i]); asm volatile("" : "+v"(o[i])); }
        asm volatile("" : "+v"(o[0]), "+v"(o[1]), "+v"(o[2]), "+v"(o[3]));
        RED5(XOR1) RED5(ROR4) RED5(ROR8)
#undef RED5
        const float inv = rcp(o[4]);
        m0 = pk(o[0] * inv, o[1] * inv); m1 = pk(o[2] * inv, o[3] * inv);
      } else {
        const LAS float* red = lf + L_RED;
        const int cc = 2 * wave + (g >> 1);
        const int src = ((lane >> 2) & 3) * 256 + (cc & 3) * 64 + ((lane & 1) * 2 + (cc >> 2)) * 16 + (g & 1) * 8 + (lane & 2);
        const f32x2 h0 = *(const LAS f32x2*)(red + src), h1 = *(const LAS f32x2*)(red + src + 4);
        float h[4] = {h0[0], h0[1], h1[0], h1[1]};
#pragma unroll
        for (int i = 0; i < 4; ++i) {
          h[i] += dppf<ROR4>(h[i]); h[i] += dppf<ROR8>(h[i]);
          const float own = rbf(h[i]), oth = dppf<XOR1>(own);
          const float gt = odd ? oth : own, up = odd ? own : oth;
          h[i] = rbf(silu(gt)) * up;
        }
        m0 = pk(h[0], h[1]); m1 = pk(h[2], h[3]);
      }
      f32x4 y[5];
      const u32x4 b0 = build_b(m0, sel, 0), b1 = build_b(m0, sel, 1), b2 = build_b(m1, sel, 0), b3 = build_b(m1, sel, 1);
#pragma unroll
      for (int cb = 0; cb < 5; ++cb) y[cb] = mfma(b0, a[cb], f32x4{0.f, 0.f, 0.f, 0.f});
#pragma unroll
      for (int cb = 0; cb < 5; ++cb) y[cb] = mfma(b1, a[5 + cb], y[cb]);
      SBAR();
#pragma unroll
      for (int cb = 0; cb < 5; ++cb) y[cb] = mfma(b2, a[10 + cb], y[cb]);
      SBAR();
#pragma unroll
      for (int cb = 0; cb < 5; ++cb) { y[cb] = mfma(b3, a[15 + cb], y[cb]); SBAR(); }
#pragma unroll
      for (int cb = 0; cb < 5; ++cb) *(LAS f32x4*)((LAS char*)lds + yd + cb * 1280) = y[cb];
      wg_barrier();
      const LAS f32x4* p = (const LAS f32x4*)((LAS char*)lds + ys);
      const f32x4 t0 = p[0], t1 = p[400], t2 = p[800], t3 = p[1200], t4 = p[320], t5 = p[720], t6 = p[1120], t7 = p[1520];
      stsc(rw, po, pack4((t0 + t1) + (t2 + t3), tag));
      stsc(rw, po2, pack4((t4 + t5) + (t6 + t7), tag));
      STAMP(30);
    }
    STAMP_OUT();
    return;
  }

  const int aw = wave - 4, c = lane & 15;
  if (__builtin_expect(bid < Q0 + NQ, 1)) {
    u32 vo = (144 * aw + 36 * g + (c >> 3)) * 8192 + bid * 128 + (c & 7) * 16, xo = ((lane >> 5) * 288 + (lane & 31) * 3) * 4;
    asm volatile("" : "+v"(vo), "+v"(xo));
    ARGS()
    const rsrc_t rx = mkrsrc(x, 2 * DM * 2), rq = mkrsrc(wq, DM * HQ * DH * 2);
    u32 xd[9]; u32x4 a[18];
    load_x(rx, xo, xd);
#pragma unroll
    for (int k = 0; k < QFIRST; ++k) a[k] = ldnt(rq, vo, k * 16384);
    SBAR();
    STAMP2(24);
    if (aw == 0 && lane == 0) lds[L_CTR] = 0u;
    const Sel sel = make_sel(lane);
    u32 xm[18];
    layernorm(xd, lds + L_XN + aw * 576, lane, aw, xm);
    STAMP2(1);
    SBAR();
    f32x4 d0 = {0.f, 0.f, 0.f, 0.f}, d1 = d0;
    u32 e = 0;
#pragma unroll
    for (int k = 0; k < 18; ++k) {
      if (k + QFIRST < 18) a[k + QFIRST] = ldnt(rq, vo, (k + QFIRST) * 16384);
      if (k + QFIRST == 18) { e = __builtin_amdgcn_raw_buffer_load_b32(rw, WS_E, 0, AUX_SC1); SBAR(); wg_barrier(); STAMP2(25); }
      SBAR();
      d0 = mfma(a[k], build_b(xm[k], sel, 0), d0); d1 = mfma(a[k], build_b(xm[k], sel, 1), d1);
      SBAR();
    }
    LAS float* red = (LAS float*)lds + L_RED;
#pragma unroll
    for (int i = 0; i < 4; ++i) red[(aw * 4 + i) * 64 + lane] = lane < 32 ? d0[i] : d1[i];
    lds_fence();
    STAMP2(2);
    u32 ticket = 0;
    if (lane == 0) ticket = __hip_atomic_fetch_add(lds + L_CTR, 1u, __ATOMIC_RELAXED, __HIP_MEMORY_SCOPE_WORKGROUP);
    if (__builtin_amdgcn_readfirstlane(ticket) == 3u) {
      asm volatile("" ::: "memory");
      const u32 tag = next_tag(__builtin_amdgcn_readfirstlane(e));
      const int bt = lane >> 5, cp = lane & 31, cc = cp >> 2;
      const int src = (cc & 3) * 64 + (cc >> 2) * 16 + (cp & 3) * 4 + 2 * bt;
      f32x2 s = {0.f, 0.f};
#pragma unroll
      for (int w = 0; w < 4; ++w) s += *(const LAS f32x2*)(red + w * 256 + src) + *(const LAS f32x2*)(red + w * 256 + src + 32);
      __builtin_amdgcn_raw_buffer_store_b64(u32x2{pk(s[0], s[1]), tag}, rw, WS_QG + (bt * 2048 + bid * 32 + cp) * 8, 0, AUX_SC1);
    }
    STAMP(6);
    if (aw >= 2) { STAMP_OUT(); return; }
  }
  ARGS()
  if (bid >= F0 && bid < F0 + NF) {
    const int u = bid - F0;
    const rsrc_t rx = mkrsrc(x, 2 * DM * 2);
    u32 xd[9];
    load_x(rx, ((lane >> 5) * 288 + (lane & 31) * 3) * 4, xd);
    const GAS u32x4* gp = (const GAS u32x4*)((const GAS char*)(c < 8 ? wg : wu) + (size_t)(144 * aw + 36 * g) * (FF * 2) + u * 128 + (c & 7) * 16);
    u32x4 ring[NAUX];
#pragma unroll
    for (int k = 0; k < NAUX; ++k) ring[k] = __builtin_nontemporal_load(gp + k * (FF * 2 / 16));
    SBAR();
    const Sel sel = make_sel(lane);
    u32 xm[18];
    layernorm(xd, lds + L_XN + aw * 576, lane, aw, xm);
    SBAR();
    f32x4 d = {0.f, 0.f, 0.f, 0.f};
#pragma unroll
    for (int k = 0; k < 36; ++k) {
      d = mfma(ring[k % NAUX], build_b(xm[k >> 1], sel, k & 1), d);
      SBAR();
      if (k + NAUX < 36) ring[k % NAUX] = __builtin_nontemporal_load(gp + (k + NAUX) * (FF * 2 / 16));
      SBAR();
    }
    LAS float* red = (LAS float*)lds + L_RED;
#pragma unroll
    for (int i = 0; i < 4; ++i) red[(aw * 4 + i) * 64 + lane] = d[i];
    STAMP(1);
    STAMP_OUT();
    wg_barrier();
    return;
  }
  if ((bid < NQ || (bid >= Z0 && bid < Z0 + 8)) && aw < 2) {
    const int f = bid < NQ ? bid : bid - (Z0 - NQ);
    const u32 oi = (lane >> 5) * DM + 8 * f + 2 * (lane & 3) + (g & 1);
    u32 xv = ((const GAS u16*)x)[oi];
    GAS u16* op = (GAS u16*)out + oi;
    u32 go = (aw ? WS_FG : WS_YG) + (f >> 1) * (NF * 128) + (lane >> 2) * 128 + (f & 1) * 64 + (lane & 3) * 16;
    asm volatile("" : "+v"(xv), "+v"(op), "+v"(go));
    wg_barrier();
    STAMPZ(31);
    const u32 tag = lds[L_TAG];
    LAS float* lg = (LAS float*)lds + L_RED + lane;
    u32x4 r[5];
    if (aw) {
      if (DLY_ZF) __builtin_amdgcn_s_sleep(DLY_ZF);
      _Pragma("nounroll") for (u32 spins = SPIN_CAP; spins; --spins) {
        RELOAD();
#pragma unroll
        for (int k = 0; k < 5; ++k) r[k] = ldsc(rw, (k == 4 && lane >= 32) ? OOB : go, k * 2048);
        if (lane >= 32) { r[4][1] = tag << 16; r[4][3] = tag << 16; }
        if (tags16_ok<5>(r, tag)) break;
      }
      *lg = rbf(silu(rbf(sum_parts<5>(r))));
      STAMPZ(7);
      wg_barrier();
      STAMPZ_OUT();
      return;
    }
    if (DLY_ZY) __builtin_amdgcn_s_sleep(DLY_ZY);
    _Pragma("nounroll") for (u32 spins = SPIN_CAP; spins; --spins) {
      RELOAD();
#pragma unroll
      for (int k = 0; k < 4; ++k) r[k] = ldsc(rw, go, k * 2048);
      if (tags16_ok<4>(r, tag)) break;
    }
    STAMP2(3);
    wg_barrier();
    float gt = *lg;
    SBAR();
    float sm = rbf(sum_parts<4>(r));
    asm volatile("" : "+v"(gt), "+v"(sm));
    const u32 res = pk(lo16(xv) + rbf(sm * gt), 0.f);
    if ((lane & 12) == 0) *op = (u16)res;
    if (f == 0 && lane == 0) __builtin_amdgcn_raw_buffer_store_b32(tag, rw, WS_E, 0, AUX_SC1);
    STAMPZ(4);
    STAMPZ_OUT();
    return;
  }
}

extern "C" void kernel_launch(void* const* d_in, const int* in_sizes, int n_in, void* d_out, int out_size, void* d_ws, size_t ws_size,
                              hipStream_t stream) {
  fused_decode<<<NWG, NTHR, 0, stream>>>((const u16*)d_in[1], (const u16*)d_in[2], (unsigned char*)d_ws, (const u16*)d_in[0], (const u16*)d_in[3],
                                          (const u16*)d_in[4], (const u16*)d_in[6], (const u16*)d_in[7], (const u16*)d_in[8], (u16*)d_out);
}
```

```cpp
#define PIN 3
#include <hip/hip_runtime.h>
#include <hip/hip_bf16.h>
#include <stdint.h>

typedef unsigned u32;
typedef unsigned short u16;
typedef u32 u32x2 __attribute__((ext_vector_type(2)));
typedef u32 u32x3 __attribute__((ext_vector_type(3)));
typedef u32 u32x4 __attribute__((ext_vector_type(4)));
typedef float f32x2 __attribute__((ext_vector_type(2)));
typedef float f32x4 __attribute__((ext_vector_type(4)));
typedef __bf16 bf16x8 __attribute__((ext_vector_type(8)));
typedef __bf16 bf16x2 __attribute__((ext_vector_type(2)));
typedef __amdgpu_buffer_rsrc_t rsrc_t;
#define GAS __attribute__((address_space(1)))
#define LAS __attribute__((address_space(3)))

#define NPRO 5
#define NFL 5
#define NHI 9
#define SCH_DT 220
#define SCH_SH 5
#define SCH_BIAS 32
#define SCH_IT0 2
#define SCH_TAB 0x0001114955FFull
#define LEAD_O 0
#define LEAD_F 0
#define QPOS 26
#define QFIRST 8
#define NAUX 4
#define NE0 3
#define NLATE 8
#define SPRINT 0
#define DLY_O 0
#define DLY_ZY 64
#define DLY_ZF 40
#define CODEPF_BYTES 23040
#define CODEPF 1

constexpr int DM = 576, HQ = 32, SK = 32768, DH = 128, FF = 4608;
constexpr int NWG = 256, NTHR = 512;
constexpr u32 CHUNK = 4096u * DH * 2u;
constexpr int TPW = 32;
constexpr int AUX_NT = 2, AUX_SC1 = 16;
constexpr u32 OOB = 0x80000000u;
constexpr int Q0 = 0, NQ = 64, O0 = 64, NO = 64, F0 = 128, NF = 72, Z0 = 200;
constexpr u32 WS_E = 0, WS_QG = 4096, WS_AG = WS_QG + 32768, WS_YG = WS_AG + NWG * 1024, WS_FG = WS_YG + 36 * NF * 128,
              WS_SC = WS_FG + 36 * NF * 128, SC_STR = 4352, WS_END = WS_SC + 16 * 8 * SC_STR;
constexpr u32 SPIN_CAP = 400000;
constexpr int L_CTR = 0, L_TAG = 4, L_ML = 8, L_ACC = 32, ACC_W = 352, L_XN = L_ACC + 4 * ACC_W, L_RED = L_XN + 4 * 576, L_Y = L_RED + 1024,
              L_S = L_Y + 4 * 1600, L_END = L_S + 4 * 2048;
constexpr float LOG2E = 1.4426950408889634f;

#define STAMPZ(k)
#define STAMPZ_OUT()
#define STAMP(k)
#define STAMP_OUT()
#define STAMP2(k)

__device__ __forceinline__ rsrc_t mkrsrc(const void* p, u32 bytes) { return __builtin_amdgcn_make_buffer_rsrc((void*)p, 0, bytes, 0x00020000); }
__device__ __forceinline__ u32x4 ldnt(rsrc_t r, u32 vo, u32 so) { return __builtin_amdgcn_raw_buffer_load_b128(r, vo, so, AUX_NT); }
__device__ __forceinline__ u32x4 ldsc(rsrc_t r, u32 vo, u32 so) { return __builtin_amdgcn_raw_buffer_load_b128(r, vo, so, AUX_SC1); }
__device__ __forceinline__ void stsc(rsrc_t r, u32 vo, u32x4 v) { __builtin_amdgcn_raw_buffer_store_b128(v, r, vo, 0, AUX_SC1); }
__device__ __forceinline__ f32x4 mfma(u32x4 a, u32x4 b, f32x4 c) {
  return __builtin_amdgcn_mfma_f32_16x16x32_bf16(__builtin_bit_cast(bf16x8, a), __builtin_bit_cast(bf16x8, b), c, 0, 0, 0);
}
__device__ __forceinline__ float asf(u32 x) { return __builtin_bit_cast(float, x); }
__device__ __forceinline__ u32 asu(float x) { return __builtin_bit_cast(u32, x); }
__device__ __forceinline__ u32 pk(float lo, float hi) { bf16x2 v = {(__bf16)lo, (__bf16)hi}; return __builtin_bit_cast(u32, v); }
__device__ __forceinline__ float rbf(float x) { return (float)(__bf16)x; }
__device__ __forceinline__ float lo16(u32 x) { return asf(x << 16); }
__device__ __forceinline__ float hi16(u32 x) { return asf(x & 0xffff0000u); }
template <int CTRL> __device__ __forceinline__ u32 dppu(u32 x) { return (u32)__builtin_amdgcn_mov_dpp((int)x, CTRL, 0xf, 0xf, true); }
template <int CTRL> __device__ __forceinline__ float dppf(float x) { return asf(dppu<CTRL>(asu(x))); }
constexpr int XOR1 = 0xB1, XOR2 = 0x4E, XOR7 = 0x141, ROR4 = 0x124, ROR8 = 0x128;
__device__ __forceinline__ float sum16(float x) { auto s = __builtin_amdgcn_permlane16_swap(asu(x), asu(x), false, false); return asf(s[0]) + asf(s[1]); }
__device__ __forceinline__ float sum32(float x) { auto s = __builtin_amdgcn_permlane32_swap(asu(x), asu(x), false, false); return asf(s[0]) + asf(s[1]); }
__device__ __forceinline__ float max16(float x) { auto s = __builtin_amdgcn_permlane16_swap(asu(x), asu(x), false, false); return fmaxf(asf(s[0]), asf(s[1])); }
__device__ __forceinline__ float max32(float x) { auto s = __builtin_amdgcn_permlane32_swap(asu(x), asu(x), false, false); return fmaxf(asf(s[0]), asf(s[1])); }
__device__ __forceinline__ float ex2(float x) { return __builtin_amdgcn_exp2f(x); }
__device__ __forceinline__ float rcp(float x) { return __builtin_amdgcn_rcpf(x); }
__device__ __forceinline__ float silu(float x) { return x * rcp(1.0f + ex2(-LOG2E * x)); }
__device__ __forceinline__ u32 now32() { return (u32)__builtin_amdgcn_s_memrealtime(); }
template <int N> __device__ __forceinline__ void pace() { asm volatile("s_waitcnt vmcnt(%0)" ::"n"(N - 1) : "memory"); }
template <int BIT, int EXTRA> __device__ __forceinline__ void pacev(u32 mask) {
  asm volatile("s_waitcnt vmcnt(%1)\n\ts_bitcmp1_b32 %0, %3\n\ts_cbranch_scc0 1f\n\ts_waitcnt vmcnt(%2)\n1:" ::"s"(mask), "n"(NHI - 1 + EXTRA), "n"(NFL - 1 + EXTRA), "n"(BIT) : "memory", "scc");
}
__device__ __forceinline__ u32 pace_mask(u32 now, u32 due) {
  int lev = (int)(now - due) >> SCH_SH;
  asm("s_max_i32 %0, %0, 0\n\ts_min_i32 %0, %0, 7" : "+s"(lev) : : "scc");
  return (u32)(SCH_TAB >> (8 * lev));
}
#define SBAR() __builtin_amdgcn_sched_barrier(0)
#define RELOAD() asm volatile("" ::: "memory")
__device__ __forceinline__ void lds_fence() { asm volatile("s_waitcnt lgkmcnt(0)" ::: "memory"); }
__device__ __forceinline__ void wg_barrier() { lds_fence(); __builtin_amdgcn_s_barrier(); asm volatile("" ::: "memory"); }
template <int N> __device__ __forceinline__ bool tags_ok(const u32x4* r, u32 tag) {
  bool bad = false;
#pragma unroll
  for (int i = 0; i < N; ++i) bad |= (r[i][1] != tag) | (r[i][3] != tag);
  return __builtin_amdgcn_ballot_w64(bad) == 0ull;
}
__device__ __forceinline__ u32 next_tag(u32 e) { return e * 1664525u + 1013904223u; }

struct Sched { u32 mask, due, tag, lead, ldo, sto; bool sample; u32x2 row; };

template <int W, int T> __device__ __forceinline__ u32 bank_mov(u32 old, u32 z) {
  constexpr int ctrl = W == T ? 0xE4 : (W > T ? 0x110 + 4 * (W - T) : 0x100 + 4 * (T - W));
  return (u32)__builtin_amdgcn_update_dpp((int)old, (int)z, ctrl, 0xf, 1 << W, false);
}
template <int T> __device__ __forceinline__ void build_p(u32x4& b, u32 z) {
  b[0] = bank_mov<0, T>(b[0], z); b[1] = bank_mov<1, T>(b[1], z); b[2] = bank_mov<2, T>(b[2], z); b[3] = bank_mov<3, T>(b[3], z);
}

template <int GRP> __device__ __forceinline__ void sched_step(Sched& sc, u32 now, rsrc_t rw, int it) {
  if (GRP == 0) {
    sc.row = __builtin_amdgcn_raw_buffer_load_b64(rw, sc.ldo, it * (8 * SC_STR), AUX_SC1);
    if (sc.sample) __builtin_amdgcn_raw_buffer_store_b64(u32x2{now + sc.lead, sc.tag}, rw, sc.sto, (it + 1) * (8 * SC_STR), AUX_SC1);
  } else {
    const bool ok = __builtin_amdgcn_ballot_w64(sc.row[1] == sc.tag) == 0xffull;
    u32 d = sc.row[0] - now;
    d += dppu<XOR1>(d); d += dppu<XOR2>(d); d += dppu<XOR7>(d);
    const u32 due = now + ((int)__builtin_amdgcn_readfirstlane(d) >> 3) + (3 * SCH_DT - SCH_BIAS) - sc.lead;
    if (ok && it >= SCH_IT0) sc.due = due;
  }
  sc.mask = pace_mask(now, sc.due);
  if (SPRINT && GRP == 2 && it == 14) sc.mask = 0u;
  sc.due += SCH_DT;
}
template <int GRP, int I>
__device__ __forceinline__ void refill(u32x4& slot, rsrc_t r, u32 vo, u32 so, Sched& sc, u32& now, rsrc_t rw, int it) {
  pacev<I, (GRP == 0 && I >= 2 && I < 2 + NFL) ? 1 : 0>(sc.mask);
  slot = ldnt(r, vo, so + GRP * 32768 + I * 1024);
  if (!(GRP & 1) && I == 0) now = now32();
  if (!(GRP & 1) && I == 1) sched_step<GRP>(sc, now, rw, it);
  SBAR();
}
template <int T> __device__ __forceinline__ void k_pick(u32x4& u, f32x4 d) {
  u[0] = bank_mov<T, 0>(u[0], asu(d[0])); u[1] = bank_mov<T, 1>(u[1], asu(d[1])); u[2] = bank_mov<T, 2>(u[2], asu(d[2])); u[3] = bank_mov<T, 3>(u[3], asu(d[3]));
}
template <int GRP>
__device__ __forceinline__ float k_tile(u32x4 (&k)[8], u32x4 bq0, u32x4 bq1, u32x4& u, rsrc_t r, u32 vo, u32 so, Sched& sc, rsrc_t rw, int it) {
  const f32x4 z = {0.f, 0.f, 0.f, 0.f};
  f32x4 da, db;
  u32 now = 0;
#define RF(I) SBAR(); refill<GRP, I>(k[I], r, vo, so, sc, now, rw, it);
  da = mfma(k[0], bq0, z);                     RF(0)
  da = mfma(k[1], bq1, da);                    RF(1)
  db = mfma(k[2], bq0, z);                     RF(2)
  db = mfma(k[3], bq1, db); k_pick<0>(u, da);  RF(3)
  da = mfma(k[4], bq0, z);                     RF(4)
  da = mfma(k[5], bq1, da); k_pick<1>(u, db);  RF(5)
  db = mfma(k[6], bq0, z);                     RF(6)
  db = mfma(k[7], bq1, db); k_pick<2>(u, da);  RF(7)
#undef RF
  k_pick<3>(u, db);
  const float s = (asf(u[0]) + asf(u[1])) + (asf(u[2]) + asf(u[3]));
  return rbf(rbf(s) * 0.08838834764831845f);
}
__device__ __forceinline__ u32x2 probs(float s, float ml, float& l, u32 hm) {
  const float p = ex2(fmaf(s, LOG2E, -ml));
  l += p;
  const u32 t = pk(p, p);
  return u32x2{dppu<0xA0>(t) & hm, dppu<0xF5>(t) & hm};
}
template <int GRP>
__device__ __forceinline__ void v_tile(u32x4 (&v)[8], u32x2 z, f32x4& acc, u32x4& bp0, u32x4& bp1, rsrc_t r, u32 vo, u32 so, Sched& sc, rsrc_t rw, int it) {
  u32 now = 0;
#define PV_STEP(U, BP) build_p<(U) / 2>(BP, z[(U) & 1]); acc = mfma(v[U], BP, acc); SBAR(); refill<GRP, U>(v[U], r, vo, so, sc, now, rw, it);
  PV_STEP(0, bp0) PV_STEP(1, bp1) PV_STEP(2, bp0) PV_STEP(3, bp1) PV_STEP(4, bp0) PV_STEP(5, bp1) PV_STEP(6, bp0) PV_STEP(7, bp1)
#undef PV_STEP
}
template <int K> __device__ __forceinline__ u32x4 row_load(rsrc_t r, u32 vo, u32 vo4) { return ldnt(r, K % 5 == 4 ? vo4 : vo, (K / 5) * 1152 + (K % 5) * 256); }
template <int FROM, int CNT, int K0>
__device__ __forceinline__ void v_last(const u32x4 (&v)[8], u32x2 z, f32x4& acc, u32x4& bp0, u32x4& bp1, u32x4 (&a)[20], rsrc_t r, u32 vo, u32 vo4) {
#define PV_STEP(U, BP) build_p<(U) / 2>(BP, z[(U) & 1]); acc = mfma(v[U], BP, acc); SBAR(); \
  if constexpr (U >= FROM && U < FROM + CNT) { a[K0 + U - FROM] = row_load<K0 + U - FROM>(r, vo, vo4); SBAR(); }
  PV_STEP(0, bp0) PV_STEP(1, bp1) PV_STEP(2, bp0) PV_STEP(3, bp1) PV_STEP(4, bp0) PV_STEP(5, bp1) PV_STEP(6, bp0) PV_STEP(7, bp1)
#undef PV_STEP
}
template <int LO, int HI> __device__ __forceinline__ void rows_load(u32x4 (&a)[20], rsrc_t r, u32 vo, u32 vo4) {
  if constexpr (LO < HI) { pace<NLATE>(); a[LO] = row_load<LO>(r, vo, vo4); SBAR(); rows_load<LO + 1, HI>(a, r, vo, vo4); }
}

struct Sel { u32 s[4][2]; };
__device__ __forceinline__ Sel make_sel(int lane) {
  Sel r; const int wp = (lane >> 2) & 3; const bool hb = lane & 1;
#pragma unroll
  for (int par = 0; par < 2; ++par) {
    const u32 two = ((2 * par + 1) << 8) | (2 * par);
    const u32 base = hb ? ((two << 16) | 0x0c0cu) : (0x0c0c0000u | two);
#pragma unroll
    for (int w = 0; w < 4; ++w) r.s[w][par] = wp == w ? base : 0x0c0c0c0cu;
  }
  return r;
}
__device__ __forceinline__ void pin_sel(Sel& s) {
  asm volatile("" : "+v"(s.s[0][0]), "+v"(s.s[0][1]), "+v"(s.s[1][0]), "+v"(s.s[1][1]), "+v"(s.s[2][0]), "+v"(s.s[2][1]), "+v"(s.s[3][0]), "+v"(s.s[3][1]));
}
__device__ __forceinline__ u32x4 build_b(u32 d, const Sel& sel, int par) {
  u32x4 b;
#pragma unroll
  for (int w = 0; w < 4; ++w) b[w] = __builtin_amdgcn_perm(d, d, sel.s[w][par]);
  return b;
}
__device__ __forceinline__ void load_x(rsrc_t rx, u32 xo, u32 (&xd)[9]) {
#pragma unroll
  for (int k = 0; k < 3; ++k) {
    const u32x3 t = __builtin_amdgcn_raw_buffer_load_b96(rx, xo, k * 384, 0);
    xd[3 * k] = t[0]; xd[3 * k + 1] = t[1]; xd[3 * k + 2] = t[2];
  }
}
__device__ __forceinline__ void layernorm(const u32 (&xd)[9], LAS u32* xn, int lane, int aw, u32 (&xm)[18]) {
  f32x2 v[9], s2 = {0.f, 0.f};
#pragma unroll
  for (int k = 0; k < 9; ++k) { v[k] = f32x2{lo16(xd[k]), hi16(xd[k])}; s2 += v[k]; }
  float s = s2[0] + s2[1];
  s += dppf<XOR1>(s); s += dppf<XOR2>(s); s += dppf<ROR4>(s); s += dppf<ROR8>(s); s = sum16(s);
  const float mean = s * (1.0f / DM);
  f32x2 q2 = {0.f, 0.f};
#pragma unroll
  for (int k = 0; k < 9; ++k) { const f32x2 a = v[k] - mean; q2 += a * a; }
  float q = q2[0] + q2[1];
  q += dppf<XOR1>(q); q += dppf<XOR2>(q); q += dppf<ROR4>(q); q += dppf<ROR8>(q); q = sum16(q);
  const float mb = rbf(mean), den = rbf(__builtin_amdgcn_sqrtf(rbf(rbf(q * (1.0f / DM)) + 1e-5f))), inv = rcp(den);
#pragma unroll
  for (int k = 0; k < 9; ++k) {
    const f32x2 a = v[k] - mb;
    const u32 r = pk(a[0], a[1]);
    const f32x2 b = f32x2{lo16(r), hi16(r)} * inv;
    xn[(lane >> 5) * 288 + (lane & 31) * 3 + 96 * (k / 3) + k % 3] = pk(b[0], b[1]);
  }
  lds_fence();
  const LAS u32x2* src = (const LAS u32x2*)(xn + ((lane >> 1) & 1) * 288 + 72 * aw + 18 * (lane >> 4));
#pragma unroll
  for (int k = 0; k < 9; ++k) { const u32x2 t = src[k]; xm[2 * k] = t[0]; xm[2 * k + 1] = t[1]; }
}
__device__ __forceinline__ u32x4 pack4(f32x4 v, u32 tag) {
  return u32x4{__builtin_amdgcn_perm(asu(v[1]), asu(v[0]), 0x05030201u), __builtin_amdgcn_perm(tag, asu(v[1]), 0x05040302u),
               __builtin_amdgcn_perm(asu(v[3]), asu(v[2]), 0x05030201u), __builtin_amdgcn_perm(tag, asu(v[3]), 0x05040302u)};
}
__device__ __forceinline__ f32x4 unpack4(u32x4 r) {
  return f32x4{asf(r[0] << 8), asf(__builtin_amdgcn_perm(r[1], r[0], 0x0504030cu)), asf(r[2] << 8), asf(__builtin_amdgcn_perm(r[3], r[2], 0x0504030cu))};
}
template <int N> __device__ __forceinline__ bool tags16_ok(const u32x4* r, u32 tag) {
  u32 bad = 0;
#pragma unroll
  for (int i = 0; i < N; ++i) bad |= (r[i][1] ^ (tag << 16)) | (r[i][3] ^ (tag << 16));
  return __builtin_amdgcn_ballot_w64(bad > 0xffffu) == 0ull;
}
template <int N> __device__ __forceinline__ float sum_parts(const u32x4* r) {
  f32x4 a = unpack4(r[0]);
#pragma unroll
  for (int i = 1; i < N; ++i) a += unpack4(r[i]);
  const auto s0 = __builtin_amdgcn_permlane32_swap(asu(a[0]), asu(a[2]), false, false), s1 = __builtin_amdgcn_permlane32_swap(asu(a[1]), asu(a[3]), false, false);
  const auto s2 = __builtin_amdgcn_permlane16_swap(asu(asf(s0[0]) + asf(s0[1])), asu(asf(s1[0]) + asf(s1[1])), false, false);
  float t = asf(s2[0]) + asf(s2[1]);
  t += dppf<ROR4>(t); t += dppf<ROR8>(t);
  return t;
}

typedef u32 u32x16 __attribute__((ext_vector_type(16)));
__device__ __forceinline__ const char* mkptr(u32 lo, u32 hi) { return (const char*)(GAS char*)(((uint64_t)hi << 32) | lo); }

__global__ void __launch_bounds__(NTHR, 2)
fused_decode(const u16* __restrict__, const u16* __restrict__, unsigned char*, const u16* __restrict__, const u16* __restrict__,
             const u16* __restrict__, const u16* __restrict__, const u16* __restrict__, const u16* __restrict__, u16*) {
  __shared__ __attribute__((aligned(16))) u32 smem[L_END];
  LAS u32* lds = (LAS u32*)smem;
  const int lane = threadIdx.x & 63, wave = __builtin_amdgcn_readfirstlane(threadIdx.x >> 6), bid = blockIdx.x;
  {
    u32 pf; uint64_t pc;
    asm volatile("s_getpc_b64 %1\n\ts_cmp_lt_u32 %3, 4\n\ts_cbranch_scc0 1f\n\tglobal_load_dword %0, %2, %1\n\ts_waitcnt vmcnt(0)\n1:"
                 : "=&v"(pf), "=&s"(pc) : "v"(min((threadIdx.x & 255u) * 128u, (u32)CODEPF_BYTES)), "s"((u32)(wave - 4) | ((u32)((bid >> 4) - (O0 >> 4)) << 4)) : "scc");
  }
  const u32 tstart = now32();
  const int g = lane >> 4;
  STAMPZ(0);
#define ARGS() \
  u32x16 ar; u32x4 ar2; \
  asm volatile("s_load_dwordx16 %0, %2, 0x0\n\ts_load_dwordx4 %1, %2, 0x40\n\ts_waitcnt lgkmcnt(0)" : "=&s"(ar), "=&s"(ar2) : "s"(__builtin_amdgcn_kernarg_segment_ptr())); \
  const char *kc = mkptr(ar[0], ar[1]), *vc = mkptr(ar[2], ar[3]), *x = mkptr(ar[6], ar[7]), *wq = mkptr(ar[8], ar[9]), *wo = mkptr(ar[10], ar[11]), \
             *wg = mkptr(ar[12], ar[13]), *wu = mkptr(ar[14], ar[15]), *wd = mkptr(ar2[0], ar2[1]); \
  char *ws = (char*)mkptr(ar[4], ar[5]), *out = (char*)mkptr(ar2[2], ar2[3]); \
  const rsrc_t rw = mkrsrc(ws, WS_END);

  if (__builtin_expect(wave < 4, 1)) {
    ARGS()
    const u32 ko = ((lane >> 2) & 3) * 256 + (4 * g + (lane & 3)) * 16;
    const rsrc_t rk = mkrsrc((const char*)kc + (size_t)bid * CHUNK, CHUNK), rv = mkrsrc((const char*)vc + (size_t)bid * CHUNK, CHUNK);
    const bool odd = lane & 1;
    const u32 vo = lane * 16;
    u32x4 r0[8], r1[8], r2[8], r3[8], qg;
    u32 so = wave * 8192, e = 0, qo = 0;
    if (bid < Q0 + NQ) wg_barrier();
#pragma unroll
    for (int p = 0; p < 32; ++p) {
      if (p == NPRO) STAMP2(24);
      if (p == NPRO) {
        u32 ln = lane; asm volatile("" : "+v"(ln));
        qo = WS_QG + (bid >> 3) * 1024 + ((ln >> 1) & 1) * 512 + (ln >> 2) * 32 + (ln & 1) * 16;
      }
      if (p == QPOS) {
        e = __builtin_amdgcn_raw_buffer_load_b32(rw, WS_E, 0, AUX_SC1);
        qg = ldsc(rw, qo, 0);
      }
      if (p >= QPOS && p < QPOS + NPRO) pace<NPRO + 2>();
      else if (p >= NPRO) pace<NPRO>();
      if (p == NPRO) STAMP2(26);
      if (p == 8 + NPRO) STAMP2(28);
      if (p == 16 + NPRO) STAMP2(29);
      const u32x4 t = ldnt(rk, ko, so + (p >> 3) * 32768 + (p & 7) * 1024);
      if (p < 8) r0[p] = t; else if (p < 16) r1[p - 8] = t; else if (p < 24) r2[p - 16] = t; else r3[p - 24] = t;
      SBAR();
    }
    STAMP(1);
    const u32 tag = next_tag(__builtin_amdgcn_readfirstlane(e));
    if (!tags_ok<1>(&qg, tag)) {
      u32 spins = SPIN_CAP;
      _Pragma("nounroll") do { RELOAD(); qg = ldsc(rw, qo, 0); } while (!tags_ok<1>(&qg, tag) && --spins);
    }
    STAMP(2);
    Sched sc;
    sc.mask = 0xffu; sc.due = now32() + 0x20000000u; sc.tag = tag;
    sc.lead = (bid >= O0 && bid < F0) ? LEAD_O : ((bid >= F0 && bid < Z0) ? LEAD_F : 0);
    sc.ldo = lane < 8 ? WS_SC + lane * SC_STR : OOB;
    sc.sample = bid == 33 * (bid & 7) && wave == 0;
    sc.sto = lane == 0 ? WS_SC + (bid & 7) * SC_STR : OOB;
    LAS f32x4* sl = (LAS f32x4*)(lds + L_S) + wave * 512 + lane;
    float ml;
    {
      const u32 p0 = dppu<XOR1>(qg[0]), p1 = dppu<XOR1>(qg[2]);
      const u32x4 bq0 = {odd ? 0u : qg[0], odd ? 0u : qg[2], odd ? 0u : p0, odd ? 0u : p1}, bq1 = {odd ? p0 : 0u, odd ? p1 : 0u, odd ? qg[0] : 0u, odd ? qg[2] : 0u};
      u32x4 u = {0u, 0u, 0u, 0u};
      float mx = -INFINITY;
      for (int it = 0; it < 8; ++it) {
        int itv = it; asm volatile("" : "+s"(itv));
        const bool last = itv == 7;
        const rsrc_t r = mkrsrc((last ? vc : kc) + (size_t)bid * CHUNK, CHUNK);
        const u32 lo = last ? vo : ko;
        so = last ? wave * 8192 : so + 131072;
        f32x4 s4;
        s4[0] = k_tile<0>(r0, bq0, bq1, u, r, lo, so, sc, rw, it);
        s4[1] = k_tile<1>(r1, bq0, bq1, u, r, lo, so, sc, rw, it);
        s4[2] = k_tile<2>(r2, bq0, bq1, u, r, lo, so, sc, rw, it);
        s4[3] = k_tile<3>(r3, bq0, bq1, u, r, lo, so, sc, rw, it);
        sl[it * 64] = s4;
        mx = fmaxf(fmaxf(mx, fmaxf(s4[0], s4[1])), fmaxf(s4[2], s4[3]));
        STAMP(8 + it);
      }
      mx = fmaxf(mx, dppf<XOR1>(mx)); mx = fmaxf(mx, dppf<ROR4>(mx)); mx = fmaxf(mx, dppf<ROR8>(mx));
      ml = max32(max16(mx)) * LOG2E;
    }
    float l = 0.f;
    f32x4 acc = {0.f, 0.f, 0.f, 0.f};
    u32x4 bp0 = {0u, 0u, 0u, 0u}, bp1 = {0u, 0u, 0u, 0u};
    const u32 hm = odd ? 0xffff0000u : 0x0000ffffu;
    f32x4 s4 = sl[0];
    for (int it = 8; it < 15; ++it) {
      so += 131072;
      const f32x4 sn = sl[(it - 7) * 64];
      v_tile<0>(r0, probs(s4[0], ml, l, hm), acc, bp0, bp1, rv, vo, so, sc, rw, it);
      v_tile<1>(r1, probs(s4[1], ml, l, hm), acc, bp0, bp1, rv, vo, so, sc, rw, it);
      v_tile<2>(r2, probs(s4[2], ml, l, hm), acc, bp0, bp1, rv, vo, so, sc, rw, it);
      v_tile<3>(r3, probs(s4[3], ml, l, hm), acc, bp0, bp1, rv, vo, so, sc, rw, it);
      s4 = sn;
      STAMP(8 + it);
    }
    const bool isO = bid >= O0 && bid < F0, isF = bid >= F0 && bid < Z0;
    const int u = bid - (isF ? F0 : O0), c = lane & 15;
    const rsrc_t rl = mkrsrc(isF ? wd : wo, isF ? FF * DM * 2 : (isO ? HQ * DH * DM * 2 : 0));
    const u32 wv = (64 * u + 16 * wave + 4 * g) * 1152 + c * 16, wv4 = c < 8 ? wv : OOB;
    u32x4 a[20];
    v_last<0, NE0, 0>(r0, probs(s4[0], ml, l, hm), acc, bp0, bp1, a, rl, wv, wv4);
    v_last<0, 0, 0>(r1, probs(s4[1], ml, l, hm), acc, bp0, bp1, a, rl, wv, wv4);
    v_last<0, 0, 0>(r2, probs(s4[2], ml, l, hm), acc, bp0, bp1, a, rl, wv, wv4);
    const u32x2 z3 = probs(s4[3], ml, l, hm);
    l += dppf<XOR1>(l); l += dppf<ROR4>(l); l += dppf<ROR8>(l); l = sum32(sum16(l));
    LAS float* lf = (LAS float*)lds;
    if (lane < 4) lf[L_ML + lane * 4 + wave] = odd ? l : ml;
    lds[L_TAG] = tag;
    u32 ad = (L_ACC + wave * ACC_W + ((lane >> 1) & 1) * 176 + g * 40 + ((lane >> 2) & 3) * 2 + (lane & 1)) * 4;
    u32 as = (L_ACC + (lane >> 5) * 176 + ((lane >> 3) & 3) * 40 + (lane & 7) * 4) * 4, am = (L_ML + (lane >> 5) * 8) * 4;
    u32 ao = WS_AG + (((bid >> 3) & 15) * 16 + (lane >> 2)) * 1024 + (((bid >> 1) & 3) * 4 + (bid >> 7) * 2 + (bid & 1)) * 64 + (lane & 3) * 16;
    asm volatile("" : "+v"(ao), "+v"(ad), "+v"(as), "+v"(am));
    SBAR();
    v_last<3, 5, NE0>(r3, z3, acc, bp0, bp1, a, rl, wv, wv4);
    STAMP2(3);
#pragma unroll
    for (int i = 0; i < 4; ++i) *(LAS float*)((LAS char*)lds + ad + 32 * i) = acc[i];
    wg_barrier();
    STAMP(4);
    if (wave == 0) {
      const f32x4 mw = *(const LAS f32x4*)((LAS char*)lds + am), lw = *(const LAS f32x4*)((LAS char*)lds + am + 16);
      f32x4 pa[4];
#pragma unroll
      for (int w = 0; w < 4; ++w) pa[w] = *(const LAS f32x4*)((LAS char*)lds + as + w * (ACC_W * 4));
      SBAR();
      const float M = fmaxf(fmaxf(mw[0], mw[1]), fmaxf(mw[2], mw[3]));
      const f32x4 ew = {ex2(mw[0] - M), ex2(mw[1] - M), ex2(mw[2] - M), ex2(mw[3] - M)};
      const float L = fmaf(ew[0], lw[0], ew[1] * lw[1]) + fmaf(ew[2], lw[2], ew[3] * lw[3]);
      const f32x4 o = (ew[0] * pa[0] + ew[1] * pa[1] + ew[2] * pa[2] + ew[3] * pa[3]) * rcp(L);
      const u32 lse = asu(M + __builtin_amdgcn_logf(L));
      stsc(rw, ao, u32x4{pk(o[0], o[1]), __builtin_amdgcn_perm(tag, lse, 0x05040100u), pk(o[2], o[3]), __builtin_amdgcn_perm(tag, lse, 0x05040302u)});
    }
    STAMP2(5);
    if (isO || isF) {
      rows_load<NE0 + 5, 20>(a, rl, wv, wv4);
      Sel sel = make_sel(lane);
      u32 po = (isF ? WS_FG : WS_YG) + ((wave * 8 + (lane >> 3)) * NF + u) * 128 + (lane & 7) * 16, po2 = (wave == 0 && lane < 32) ? po + 32 * NF * 128 : OOB;
      u32 yd = L_Y * 4 + (wave * 400 + g * 20 + c) * 16, ys = L_Y * 4 + (wave * 80 + (lane & 3) * 20 + (lane >> 2)) * 16;
      pin_sel(sel);
      asm volatile("" : "+v"(po), "+v"(po2), "+v"(yd), "+v"(ys));
      STAMP(6);
      u32 m0, m1;
      if (isO) {
        const u32 go = WS_AG + (u * 4 + wave) * 1024 + c * 64 + g * 16;
        if (DLY_O) __builtin_amdgcn_s_sleep(DLY_O);
        u32x4 r;
        _Pragma("nounroll") for (u32 spins = SPIN_CAP; spins; --spins) {
          RELOAD();
          r = ldsc(rw, go, 0);
          if (tags16_ok<1>(&r, tag)) break;
        }
        STAMP2(7);
        const float lse = asf(__builtin_amdgcn_perm(r[3], r[1], 0x05040100u));
        float M = fmaxf(lse, dppf<XOR1>(lse)); M = fmaxf(M, dppf<ROR4>(M)); M = fmaxf(M, dppf<ROR8>(M));
        const float wt = ex2(lse - M);
        float o[5] = {wt * lo16(r[0]), wt * hi16(r[0]), wt * lo16(r[2]), wt * hi16(r[2]), wt};
#define RED5(C) _Pragma("unroll") for (int i = 0; i < 5; ++i) { o[i] += dppf<C>(o[i]); asm volatile("" : "+v"(o[i])); }
        asm volatile("" : "+v"(o[0]), "+v"(o[1]), "+v"(o[2]), "+v"(o[3]));
        RED5(XOR1) RED5(ROR4) RED5(ROR8)
#undef RED5
        const float inv = rcp(o[4]);
        m0 = pk(o[0] * inv, o[1] * inv); m1 = pk(o[2] * inv, o[3] * inv);
      } else {
        const LAS float* red = lf + L_RED;
        const int cc = 2 * wave + (g >> 1);
        const int src = ((lane >> 2) & 3) * 256 + (cc & 3) * 64 + ((lane & 1) * 2 + (cc >> 2)) * 16 + (g & 1) * 8 + (lane & 2);
        const f32x2 h0 = *(const LAS f32x2*)(red + src), h1 = *(const LAS f32x2*)(red + src + 4);
        float h[4] = {h0[0], h0[1], h1[0], h1[1]};
#pragma unroll
        for (int i = 0; i < 4; ++i) {
          h[i] += dppf<ROR4>(h[i]); h[i] += dppf<ROR8>(h[i]);
          const float own = rbf(h[i]), oth = dppf<XOR1>(own);
          const float gt = odd ? oth : own, up = odd ? own : oth;
          h[i] = rbf(silu(gt)) * up;
        }
        m0 = pk(h[0], h[1]); m1 = pk(h[2], h[3]);
      }
      f32x4 y[5];
      const u32x4 b0 = build_b(m0, sel, 0), b1 = build_b(m0, sel, 1), b2 = build_b(m1, sel, 0), b3 = build_b(m1, sel, 1);
#pragma unroll
      for (int cb = 0; cb < 5; ++cb) y[cb] = mfma(b0, a[cb], f32x4{0.f, 0.f, 0.f, 0.f});
#pragma unroll
      for (int cb = 0; cb < 5; ++cb) y[cb] = mfma(b1, a[5 + cb], y[cb]);
      SBAR();
#pragma unroll
      for (int cb = 0; cb < 5; ++cb) y[cb] = mfma(b2, a[10 + cb], y[cb]);
      SBAR();
#pragma unroll
      for (int cb = 0; cb < 5; ++cb) { y[cb] = mfma(b3, a[15 + cb], y[cb]); SBAR(); }
#pragma unroll
      for (int cb = 0; cb < 5; ++cb) *(LAS f32x4*)((LAS char*)lds + yd + cb * 1280) = y[cb];
      wg_barrier();
      const LAS f32x4* p = (const LAS f32x4*)((LAS char*)lds + ys);
      const f32x4 t0 = p[0], t1 = p[400], t2 = p[800], t3 = p[1200], t4 = p[320], t5 = p[720], t6 = p[1120], t7 = p[1520];
      stsc(rw, po, pack4((t0 + t1) + (t2 + t3), tag));
      stsc(rw, po2, pack4((t4 + t5) + (t6 + t7), tag));
      STAMP(30);
    }
    STAMP_OUT();
    return;
  }

  const int aw = wave - 4, c = lane & 15;
  if (__builtin_expect(bid < Q0 + NQ, 1)) {
    u32 vo = (144 * aw + 36 * g + (c >> 3)) * 8192 + bid * 128 + (c & 7) * 16, xo = ((lane >> 5) * 288 + (lane & 31) * 3) * 4;
    asm volatile("" : "+v"(vo), "+v"(xo));
    ARGS()
    const rsrc_t rx = mkrsrc(x, 2 * DM * 2), rq = mkrsrc(wq, DM * HQ * DH * 2);
    u32 xd[9]; u32x4 a[18];
    load_x(rx, xo, xd);
#pragma unroll
    for (int k = 0; k < QFIRST; ++k) a[k] = ldnt(rq, vo, k * 16384);
    SBAR();
    STAMP2(24);
    if (aw == 0 && lane == 0) lds[L_CTR] = 0u;
    const Sel sel = make_sel(lane);
    u32 xm[18];
    layernorm(xd, lds + L_XN + aw * 576, lane, aw, xm);
    STAMP2(1);
    SBAR();
    f32x4 d0 = {0.f, 0.f, 0.f, 0.f}, d1 = d0;
    u32 e = 0;
#pragma unroll
    for (int k = 0; k < 18; ++k) {
      if (k + QFIRST < 18) a[k + QFIRST] = ldnt(rq, vo, (k + QFIRST) * 16384);
      if (k + QFIRST == 18) { e = __builtin_amdgcn_raw_buffer_load_b32(rw, WS_E, 0, AUX_SC1); SBAR(); wg_barrier(); STAMP2(25); }
      SBAR();
      d0 = mfma(a[k], build_b(xm[k], sel, 0), d0); d1 = mfma(a[k], build_b(xm[k], sel, 1), d1);
      SBAR();
    }
    LAS float* red = (LAS float*)lds + L_RED;
#pragma unroll
    for (int i = 0; i < 4; ++i) red[(aw * 4 + i) * 64 + lane] = lane < 32 ? d0[i] : d1[i];
    lds_fence();
    STAMP2(2);
    u32 ticket = 0;
    if (lane == 0) ticket = __hip_atomic_fetch_add(lds + L_CTR, 1u, __ATOMIC_RELAXED, __HIP_MEMORY_SCOPE_WORKGROUP);
    if (__builtin_amdgcn_readfirstlane(ticket) == 3u) {
      asm volatile("" ::: "memory");
      const u32 tag = next_tag(__builtin_amdgcn_readfirstlane(e));
      const int bt = lane >> 5, cp = lane & 31, cc = cp >> 2;
      const int src = (cc & 3) * 64 + (cc >> 2) * 16 + (cp & 3) * 4 + 2 * bt;
      f32x2 s = {0.f, 0.f};
#pragma unroll
      for (int w = 0; w < 4; ++w) s += *(const LAS f32x2*)(red + w * 256 + src) + *(const LAS f32x2*)(red + w * 256 + src + 32);
      __builtin_amdgcn_raw_buffer_store_b64(u32x2{pk(s[0], s[1]), tag}, rw, WS_QG + (bt * 2048 + bid * 32 + cp) * 8, 0, AUX_SC1);
    }
    STAMP(6);
    if (aw >= 2) { STAMP_OUT(); return; }
  }
  ARGS()
  if (bid >= F0 && bid < F0 + NF) {
    const int u = bid - F0;
    const rsrc_t rx = mkrsrc(x, 2 * DM * 2);
    u32 xd[9];
    load_x(rx, ((lane >> 5) * 288 + (lane & 31) * 3) * 4, xd);
    const GAS u32x4* gp = (const GAS u32x4*)((const GAS char*)(c < 8 ? wg : wu) + (size_t)(144 * aw + 36 * g) * (FF * 2) + u * 128 + (c & 7) * 16);
    u32x4 ring[NAUX];
#pragma unroll
    for (int k = 0; k < NAUX; ++k) ring[k] = __builtin_nontemporal_load(gp + k * (FF * 2 / 16));
    SBAR();
    const Sel sel = make_sel(lane);
    u32 xm[18];
    layernorm(xd, lds + L_XN + aw * 576, lane, aw, xm);
    SBAR();
    f32x4 d = {0.f, 0.f, 0.f, 0.f};
#pragma unroll
    for (int k = 0; k < 36; ++k) {
      d = mfma(ring[k % NAUX], build_b(xm[k >> 1], sel, k & 1), d);
      SBAR();
      if (k + NAUX < 36) ring[k % NAUX] = __builtin_nontemporal_load(gp + (k + NAUX) * (FF * 2 / 16));
      SBAR();
    }
    LAS float* red = (LAS float*)lds + L_RED;
#pragma unroll
    for (int i = 0; i < 4; ++i) red[(aw * 4 + i) * 64 + lane] = d[i];
    STAMP(1);
    STAMP_OUT();
    wg_barrier();
    return;
  }
  if ((bid < NQ || (bid >= Z0 && bid < Z0 + 8)) && aw < 2) {
    const int f = bid < NQ ? bid : bid - (Z0 - NQ);
    const u32 oi = (lane >> 5) * DM + 8 * f + 2 * (lane & 3) + (g & 1);
    u32 xv = ((const GAS u16*)x)[oi];
    GAS u16* op = (GAS u16*)out + oi;
    u32 go = (aw ? WS_FG : WS_YG) + (f >> 1) * (NF * 128) + (lane >> 2) * 128 + (f & 1) * 64 + (lane & 3) * 16;
    asm volatile("" : "+v"(xv), "+v"(op), "+v"(go));
    wg_barrier();
    STAMPZ(31);
    const u32 tag = lds[L_TAG];
    LAS float* lg = (LAS float*)lds + L_RED + lane;
    u32x4 r[5];
    if (aw) {
      if (DLY_ZF) __builtin_amdgcn_s_sleep(DLY_ZF);
      _Pragma("nounroll") for (u32 spins = SPIN_CAP; spins; --spins) {
        RELOAD();
#pragma unroll
        for (int k = 0; k < 5; ++k) r[k] = ldsc(rw, (k == 4 && lane >= 32) ? OOB : go, k * 2048);
        if (lane >= 32) { r[4][1] = tag << 16; r[4][3] = tag << 16; }
        if (tags16_ok<5>(r, tag)) break;
      }
      *lg = rbf(silu(rbf(sum_parts<5>(r))));
      STAMPZ(7);
      wg_barrier();
      STAMPZ_OUT();
      return;
    }
    if (DLY_ZY) __builtin_amdgcn_s_sleep(DLY_ZY);
    _Pragma("nounroll") for (u32 spins = SPIN_CAP; spins; --spins) {
      RELOAD();
#pragma unroll
      for (int k = 0; k < 4; ++k) r[k] = ldsc(rw, go, k * 2048);
      if (tags16_ok<4>(r, tag)) break;
    }
    STAMP2(3);
    wg_barrier();
    float gt = *lg;
    SBAR();
    float sm = rbf(sum_parts<4>(r));
    asm volatile("" : "+v"(gt), "+v"(sm));
    const u32 res = pk(lo16(xv) + rbf(sm * gt), 0.f);
    if ((lane & 12) == 0) *op = (u16)res;
    if (f == 0 && lane == 0) __builtin_amdgcn_raw_buffer_store_b32(tag, rw, WS_E, 0, AUX_SC1);
    if (f == 0) {
      if (PIN == 2 && lane == 0) *(GAS u32*)(ws + (8u << 20)) = tstart;
      if (PIN == 1 || PIN == 3) {
        const u32 t0 = PIN == 1 ? tstart : __builtin_amdgcn_readfirstlane(*(GAS u32*)(ws + (8u << 20)));
        while (now32() - t0 < 30000u) __builtin_amdgcn_s_sleep(1);
      }
    }
    STAMPZ(4);
    STAMPZ_OUT();
    return;
  }
}

__global__ void tiny_pre(u32* p) { *p = now32(); }
__global__ void tiny_post(const u32* p) { const u32 t0 = *p; while (now32() - t0 < 30000u) __builtin_amdgcn_s_sleep(1); }
extern "C" void kernel_launch(void* const* d_in, const int* in_sizes, int n_in, void* d_out, int out_size, void* d_ws, size_t ws_size,
                              hipStream_t stream) {
  if (PIN >= 3) tiny_pre<<<1, 1, 0, stream>>>((u32*)((char*)d_ws + (8u << 20)));
  fused_decode<<<NWG, NTHR, 0, stream>>>((const u16*)d_in[1], (const u16*)d_in[2], (unsigned char*)d_ws, (const u16*)d_in[0], (const u16*)d_in[3],
                                          (const u16*)d_in[4], (const u16*)d_in[6], (const u16*)d_in[7], (const u16*)d_in[8], (u16*)d_out);
  if (PIN == 2 || PIN == 4) tiny_post<<<1, 1, 0, stream>>>((const u32*)((char*)d_ws + (8u << 20)));
}
```

```cpp
#define PROBE 1
#include <hip/hip_runtime.h>
#include <hip/hip_bf16.h>
#include <stdint.h>

typedef unsigned u32;
typedef unsigned short u16;
typedef u32 u32x2 __attribute__((ext_vector_type(2)));
typedef u32 u32x3 __attribute__((ext_vector_type(3)));
typedef u32 u32x4 __attribute__((ext_vector_type(4)));
typedef float f32x2 __attribute__((ext_vector_type(2)));
typedef float f32x4 __attribute__((ext_vector_type(4)));
typedef __bf16 bf16x8 __attribute__((ext_vector_type(8)));
typedef __bf16 bf16x2 __attribute__((ext_vector_type(2)));
typedef __amdgpu_buffer_rsrc_t rsrc_t;
#define GAS __attribute__((address_space(1)))
#define LAS __attribute__((address_space(3)))

#define NPRO 5
#define NFL 5
#define NHI 9
#define SCH_DT 220
#define SCH_SH 5
#define SCH_BIAS 32
#define SCH_IT0 2
#define SCH_TAB 0x0001114955FFull
#define LEAD_O 0
#define LEAD_F 0
#define QPOS 26
#define QFIRST 8
#define NAUX 4
#define NE0 3
#define NLATE 8
#define SPRINT 0
#define DLY_O 0
#define DLY_ZY 64
#define DLY_ZF 40
#define CODEPF_BYTES 23040
#define CODEPF 1

constexpr int DM = 576, HQ = 32, SK = 32768, DH = 128, FF = 4608;
constexpr int NWG = 256, NTHR = 512;
constexpr u32 CHUNK = 4096u * DH * 2u;
constexpr int TPW = 32;
constexpr int AUX_NT = 2, AUX_SC1 = 16;
constexpr u32 OOB = 0x80000000u;
constexpr int Q0 = 0, NQ = 64, O0 = 64, NO = 64, F0 = 128, NF = 72, Z0 = 200;
constexpr u32 WS_E = 0, WS_QG = 4096, WS_AG = WS_QG + 32768, WS_YG = WS_AG + NWG * 1024, WS_FG = WS_YG + 36 * NF * 128,
              WS_SC = WS_FG + 36 * NF * 128, SC_STR = 4352, WS_END = WS_SC + 16 * 8 * SC_STR;
constexpr u32 SPIN_CAP = 400000;
constexpr int L_CTR = 0, L_TAG = 4, L_ML = 8, L_ACC = 32, ACC_W = 352, L_XN = L_ACC + 4 * ACC_W, L_RED = L_XN + 4 * 576, L_Y = L_RED + 1024,
              L_S = L_Y + 4 * 1600, L_END = L_S + 4 * 2048;
constexpr float LOG2E = 1.4426950408889634f;

#define STAMPZ(k)
#define STAMPZ_OUT()
#define STAMP(k)
#define STAMP_OUT()
#define STAMP2(k)

__device__ __forceinline__ rsrc_t mkrsrc(const void* p, u32 bytes) { return __builtin_amdgcn_make_buffer_rsrc((void*)p, 0, bytes, 0x00020000); }
__device__ __forceinline__ u32x4 ldnt(rsrc_t r, u32 vo, u32 so) { return __builtin_amdgcn_raw_buffer_load_b128(r, vo, so, AUX_NT); }
__device__ __forceinline__ u32x4 ldsc(rsrc_t r, u32 vo, u32 so) { return __builtin_amdgcn_raw_buffer_load_b128(r, vo, so, AUX_SC1); }
__device__ __forceinline__ void stsc(rsrc_t r, u32 vo, u32x4 v) { __builtin_amdgcn_raw_buffer_store_b128(v, r, vo, 0, AUX_SC1); }
__device__ __forceinline__ f32x4 mfma(u32x4 a, u32x4 b, f32x4 c) {
  return __builtin_amdgcn_mfma_f32_16x16x32_bf16(__builtin_bit_cast(bf16x8, a), __builtin_bit_cast(bf16x8, b), c, 0, 0, 0);
}
__device__ __forceinline__ float asf(u32 x) { return __builtin_bit_cast(float, x); }
__device__ __forceinline__ u32 asu(float x) { return __builtin_bit_cast(u32, x); }
__device__ __forceinline__ u32 pk(float lo, float hi) { bf16x2 v = {(__bf16)lo, (__bf16)hi}; return __builtin_bit_cast(u32, v); }
__device__ __forceinline__ float rbf(float x) { return (float)(__bf16)x; }
__device__ __forceinline__ float lo16(u32 x) { return asf(x << 16); }
__device__ __forceinline__ float hi16(u32 x) { return asf(x & 0xffff0000u); }
template <int CTRL> __device__ __forceinline__ u32 dppu(u32 x) { return (u32)__builtin_amdgcn_mov_dpp((int)x, CTRL, 0xf, 0xf, true); }
template <int CTRL> __device__ __forceinline__ float dppf(float x) { return asf(dppu<CTRL>(asu(x))); }
constexpr int XOR1 = 0xB1, XOR2 = 0x4E, XOR7 = 0x141, ROR4 = 0x124, ROR8 = 0x128;
__device__ __forceinline__ float sum16(float x) { auto s = __builtin_amdgcn_permlane16_swap(asu(x), asu(x), false, false); return asf(s[0]) + asf(s[1]); }
__device__ __forceinline__ float sum32(float x) { auto s = __builtin_amdgcn_permlane32_swap(asu(x), asu(x), false, false); return asf(s[0]) + asf(s[1]); }
__device__ __forceinline__ float max16(float x) { auto s = __builtin_amdgcn_permlane16_swap(asu(x), asu(x), false, false); return fmaxf(asf(s[0]), asf(s[1])); }
__device__ __forceinline__ float max32(float x) { auto s = __builtin_amdgcn_permlane32_swap(asu(x), asu(x), false, false); return fmaxf(asf(s[0]), asf(s[1])); }
__device__ __forceinline__ float ex2(float x) { return __builtin_amdgcn_exp2f(x); }
__device__ __forceinline__ float rcp(float x) { return __builtin_amdgcn_rcpf(x); }
__device__ __forceinline__ float silu(float x) { return x * rcp(1.0f + ex2(-LOG2E * x)); }
__device__ __forceinline__ u32 now32() { return (u32)__builtin_amdgcn_s_memrealtime(); }
template <int N> __device__ __forceinline__ void pace() { asm volatile("s_waitcnt vmcnt(%0)" ::"n"(N - 1) : "memory"); }
template <int BIT, int EXTRA> __device__ __forceinline__ void pacev(u32 mask) {
  asm volatile("s_waitcnt vmcnt(%1)\n\ts_bitcmp1_b32 %0, %3\n\ts_cbranch_scc0 1f\n\ts_waitcnt vmcnt(%2)\n1:" ::"s"(mask), "n"(NHI - 1 + EXTRA), "n"(NFL - 1 + EXTRA), "n"(BIT) : "memory", "scc");
}
__device__ __forceinline__ u32 pace_mask(u32 now, u32 due) {
  int lev = (int)(now - due) >> SCH_SH;
  asm("s_max_i32 %0, %0, 0\n\ts_min_i32 %0, %0, 7" : "+s"(lev) : : "scc");
  return (u32)(SCH_TAB >> (8 * lev));
}
#define SBAR() __builtin_amdgcn_sched_barrier(0)
#define RELOAD() asm volatile("" ::: "memory")
__device__ __forceinline__ void lds_fence() { asm volatile("s_waitcnt lgkmcnt(0)" ::: "memory"); }
__device__ __forceinline__ void wg_barrier() { lds_fence(); __builtin_amdgcn_s_barrier(); asm volatile("" ::: "memory"); }
template <int N> __device__ __forceinline__ bool tags_ok(const u32x4* r, u32 tag) {
  bool bad = false;
#pragma unroll
  for (int i = 0; i < N; ++i) bad |= (r[i][1] != tag) | (r[i][3] != tag);
  return __builtin_amdgcn_ballot_w64(bad) == 0ull;
}
__device__ __forceinline__ u32 next_tag(u32 e) { return e * 1664525u + 1013904223u; }

struct Sched { u32 mask, due, tag, lead, ldo, sto; bool sample; u32x2 row; };

template <int W, int T> __device__ __forceinline__ u32 bank_mov(u32 old, u32 z) {
  constexpr int ctrl = W == T ? 0xE4 : (W > T ? 0x110 + 4 * (W - T) : 0x100 + 4 * (T - W));
  return (u32)__builtin_amdgcn_update_dpp((int)old, (int)z, ctrl, 0xf, 1 << W, false);
}
template <int T> __device__ __forceinline__ void build_p(u32x4& b, u32 z) {
  b[0] = bank_mov<0, T>(b[0], z); b[1] = bank_mov<1, T>(b[1], z); b[2] = bank_mov<2, T>(b[2], z); b[3] = bank_mov<3, T>(b[3], z);
}

template <int GRP> __device__ __forceinline__ void sched_step(Sched& sc, u32 now, rsrc_t rw, int it) {
  if (GRP == 0) {
    sc.row = __builtin_amdgcn_raw_buffer_load_b64(rw, sc.ldo, it * (8 * SC_STR), AUX_SC1);
    if (sc.sample) __builtin_amdgcn_raw_buffer_store_b64(u32x2{now + sc.lead, sc.tag}, rw, sc.sto, (it + 1) * (8 * SC_STR), AUX_SC1);
  } else {
    const bool ok = __builtin_amdgcn_ballot_w64(sc.row[1] == sc.tag) == 0xffull;
    u32 d = sc.row[0] - now;
    d += dppu<XOR1>(d); d += dppu<XOR2>(d); d += dppu<XOR7>(d);
    const u32 due = now + ((int)__builtin_amdgcn_readfirstlane(d) >> 3) + (3 * SCH_DT - SCH_BIAS) - sc.lead;
    if (ok && it >= SCH_IT0) sc.due = due;
  }
  sc.mask = pace_mask(now, sc.due);
  if (SPRINT && GRP == 2 && it == 14) sc.mask = 0u;
  sc.due += SCH_DT;
}
template <int GRP, int I>
__device__ __forceinline__ void refill(u32x4& slot, rsrc_t r, u32 vo, u32 so, Sched& sc, u32& now, rsrc_t rw, int it) {
  pacev<I, (GRP == 0 && I >= 2 && I < 2 + NFL) ? 1 : 0>(sc.mask);
  slot = ldnt(r, vo, so + GRP * 32768 + I * 1024);
  if (!(GRP & 1) && I == 0) now = now32();
  if (!(GRP & 1) && I == 1) sched_step<GRP>(sc, now, rw, it);
  SBAR();
}
template <int T> __device__ __forceinline__ void k_pick(u32x4& u, f32x4 d) {
  u[0] = bank_mov<T, 0>(u[0], asu(d[0])); u[1] = bank_mov<T, 1>(u[1], asu(d[1])); u[2] = bank_mov<T, 2>(u[2], asu(d[2])); u[3] = bank_mov<T, 3>(u[3], asu(d[3]));
}
template <int GRP>
__device__ __forceinline__ float k_tile(u32x4 (&k)[8], u32x4 bq0, u32x4 bq1, u32x4& u, rsrc_t r, u32 vo, u32 so, Sched& sc, rsrc_t rw, int it) {
  const f32x4 z = {0.f, 0.f, 0.f, 0.f};
  f32x4 da, db;
  u32 now = 0;
#define RF(I) SBAR(); refill<GRP, I>(k[I], r, vo, so, sc, now, rw, it);
  da = mfma(k[0], bq0, z);                     RF(0)
  da = mfma(k[1], bq1, da);                    RF(1)
  db = mfma(k[2], bq0, z);                     RF(2)
  db = mfma(k[3], bq1, db); k_pick<0>(u, da);  RF(3)
  da = mfma(k[4], bq0, z);                     RF(4)
  da = mfma(k[5], bq1, da); k_pick<1>(u, db);  RF(5)
  db = mfma(k[6], bq0, z);                     RF(6)
  db = mfma(k[7], bq1, db); k_pick<2>(u, da);  RF(7)
#undef RF
  k_pick<3>(u, db);
  const float s = (asf(u[0]) + asf(u[1])) + (asf(u[2]) + asf(u[3]));
  return rbf(rbf(s) * 0.08838834764831845f);
}
__device__ __forceinline__ u32x2 probs(float s, float ml, float& l, u32 hm) {
  const float p = ex2(fmaf(s, LOG2E, -ml));
  l += p;
  const u32 t = pk(p, p);
  return u32x2{dppu<0xA0>(t) & hm, dppu<0xF5>(t) & hm};
}
template <int GRP>
__device__ __forceinline__ void v_tile(u32x4 (&v)[8], u32x2 z, f32x4& acc, u32x4& bp0, u32x4& bp1, rsrc_t r, u32 vo, u32 so, Sched& sc, rsrc_t rw, int it) {
  u32 now = 0;
#define PV_STEP(U, BP) build_p<(U) / 2>(BP, z[(U) & 1]); acc = mfma(v[U], BP, acc); SBAR(); refill<GRP, U>(v[U], r, vo, so, sc, now, rw, it);
  PV_STEP(0, bp0) PV_STEP(1, bp1) PV_STEP(2, bp0) PV_STEP(3, bp1) PV_STEP(4, bp0) PV_STEP(5, bp1) PV_STEP(6, bp0) PV_STEP(7, bp1)
#undef PV_STEP
}
template <int K> __device__ __forceinline__ u32x4 row_load(rsrc_t r, u32 vo, u32 vo4) { return ldnt(r, K % 5 == 4 ? vo4 : vo, (K / 5) * 1152 + (K % 5) * 256); }
template <int FROM, int CNT, int K0>
__device__ __forceinline__ void v_last(const u32x4 (&v)[8], u32x2 z, f32x4& acc, u32x4& bp0, u32x4& bp1, u32x4 (&a)[20], rsrc_t r, u32 vo, u32 vo4) {
#define PV_STEP(U, BP) build_p<(U) / 2>(BP, z[(U) & 1]); acc = mfma(v[U], BP, acc); SBAR(); \
  if constexpr (U >= FROM && U < FROM + CNT) { a[K0 + U - FROM] = row_load<K0 + U - FROM>(r, vo, vo4); SBAR(); }
  PV_STEP(0, bp0) PV_STEP(1, bp1) PV_STEP(2, bp0) PV_STEP(3, bp1) PV_STEP(4, bp0) PV_STEP(5, bp1) PV_STEP(6, bp0) PV_STEP(7, bp1)
#undef PV_STEP
}
template <int LO, int HI> __device__ __forceinline__ void rows_load(u32x4 (&a)[20], rsrc_t r, u32 vo, u32 vo4) {
  if constexpr (LO < HI) { pace<NLATE>(); a[LO] = row_load<LO>(r, vo, vo4); SBAR(); rows_load<LO + 1, HI>(a, r, vo, vo4); }
}

struct Sel { u32 s[4][2]; };
__device__ __forceinline__ Sel make_sel(int lane) {
  Sel r; const int wp = (lane >> 2) & 3; const bool hb = lane & 1;
#pragma unroll
  for (int par = 0; par < 2; ++par) {
    const u32 two = ((2 * par + 1) << 8) | (2 * par);
    const u32 base = hb ? ((two << 16) | 0x0c0cu) : (0x0c0c0000u | two);
#pragma unroll
    for (int w = 0; w < 4; ++w) r.s[w][par] = wp == w ? base : 0x0c0c0c0cu;
  }
  return r;
}
__device__ __forceinline__ void pin_sel(Sel& s) {
  asm volatile("" : "+v"(s.s[0][0]), "+v"(s.s[0][1]), "+v"(s.s[1][0]), "+v"(s.s[1][1]), "+v"(s.s[2][0]), "+v"(s.s[2][1]), "+v"(s.s[3][0]), "+v"(s.s[3][1]));
}
__device__ __forceinline__ u32x4 build_b(u32 d, const Sel& sel, int par) {
  u32x4 b;
#pragma unroll
  for (int w = 0; w < 4; ++w) b[w] = __builtin_amdgcn_perm(d, d, sel.s[w][par]);
  return b;
}
__device__ __forceinline__ void load_x(rsrc_t rx, u32 xo, u32 (&xd)[9]) {
#pragma unroll
  for (int k = 0; k < 3; ++k) {
    const u32x3 t = __builtin_amdgcn_raw_buffer_load_b96(rx, xo, k * 384, 0);
    xd[3 * k] = t[0]; xd[3 * k + 1] = t[1]; xd[3 * k + 2] = t[2];
  }
}
__device__ __forceinline__ void layernorm(const u32 (&xd)[9], LAS u32* xn, int lane, int aw, u32 (&xm)[18]) {
  f32x2 v[9], s2 = {0.f, 0.f};
#pragma unroll
  for (int k = 0; k < 9; ++k) { v[k] = f32x2{lo16(xd[k]), hi16(xd[k])}; s2 += v[k]; }
  float s = s2[0] + s2[1];
  s += dppf<XOR1>(s); s += dppf<XOR2>(s); s += dppf<ROR4>(s); s += dppf<ROR8>(s); s = sum16(s);
  const float mean = s * (1.0f / DM);
  f32x2 q2 = {0.f, 0.f};
#pragma unroll
  for (int k = 0; k < 9; ++k) { const f32x2 a = v[k] - mean; q2 += a * a; }
  float q = q2[0] + q2[1];
  q += dppf<XOR1>(q); q += dppf<XOR2>(q); q += dppf<ROR4>(q); q += dppf<ROR8>(q); q = sum16(q);
  const float mb = rbf(mean), den = rbf(__builtin_amdgcn_sqrtf(rbf(rbf(q * (1.0f / DM)) + 1e-5f))), inv = rcp(den);
#pragma unroll
  for (int k = 0; k < 9; ++k) {
    const f32x2 a = v[k] - mb;
    const u32 r = pk(a[0], a[1]);
    const f32x2 b = f32x2{lo16(r), hi16(r)} * inv;
    xn[(lane >> 5) * 288 + (lane & 31) * 3 + 96 * (k / 3) + k % 3] = pk(b[0], b[1]);
  }
  lds_fence();
  const LAS u32x2* src = (const LAS u32x2*)(xn + ((lane >> 1) & 1) * 288 + 72 * aw + 18 * (lane >> 4));
#pragma unroll
  for (int k = 0; k < 9; ++k) { const u32x2 t = src[k]; xm[2 * k] = t[0]; xm[2 * k + 1] = t[1]; }
}
__device__ __forceinline__ u32x4 pack4(f32x4 v, u32 tag) {
  return u32x4{__builtin_amdgcn_perm(asu(v[1]), asu(v[0]), 0x05030201u), __builtin_amdgcn_perm(tag, asu(v[1]), 0x05040302u),
               __builtin_amdgcn_perm(asu(v[3]), asu(v[2]), 0x05030201u), __builtin_amdgcn_perm(tag, asu(v[3]), 0x05040302u)};
}
__device__ __forceinline__ f32x4 unpack4(u32x4 r) {
  return f32x4{asf(r[0] << 8), asf(__builtin_amdgcn_perm(r[1], r[0], 0x0504030cu)), asf(r[2] << 8), asf(__builtin_amdgcn_perm(r[3], r[2], 0x0504030cu))};
}
template <int N> __device__ __forceinline__ bool tags16_ok(const u32x4* r, u32 tag) {
  u32 bad = 0;
#pragma unroll
  for (int i = 0; i < N; ++i) bad |= (r[i][1] ^ (tag << 16)) | (r[i][3] ^ (tag << 16));
  return __builtin_amdgcn_ballot_w64(bad > 0xffffu) == 0ull;
}
template <int N> __device__ __forceinline__ float sum_parts(const u32x4* r) {
  f32x4 a = unpack4(r[0]);
#pragma unroll
  for (int i = 1; i < N; ++i) a += unpack4(r[i]);
  const auto s0 = __builtin_amdgcn_permlane32_swap(asu(a[0]), asu(a[2]), false, false), s1 = __builtin_amdgcn_permlane32_swap(asu(a[1]), asu(a[3]), false, false);
  const auto s2 = __builtin_amdgcn_permlane16_swap(asu(asf(s0[0]) + asf(s0[1])), asu(asf(s1[0]) + asf(s1[1])), false, false);
  float t = asf(s2[0]) + asf(s2[1]);
  t += dppf<ROR4>(t); t += dppf<ROR8>(t);
  return t;
}

typedef u32 u32x16 __attribute__((ext_vector_type(16)));
__device__ __forceinline__ const char* mkptr(u32 lo, u32 hi) { return (const char*)(GAS char*)(((uint64_t)hi << 32) | lo); }

__global__ void __launch_bounds__(NTHR, 2)
fused_decode(const u16* __restrict__, const u16* __restrict__, unsigned char*, const u16* __restrict__, const u16* __restrict__,
             const u16* __restrict__, const u16* __restrict__, const u16* __restrict__, const u16* __restrict__, u16*) {
  __shared__ __attribute__((aligned(16))) u32 smem[L_END];
  LAS u32* lds = (LAS u32*)smem;
  const int lane = threadIdx.x & 63, wave = __builtin_amdgcn_readfirstlane(threadIdx.x >> 6), bid = blockIdx.x;
  {
    u32 pf; uint64_t pc;
    asm volatile("s_getpc_b64 %1\n\ts_cmp_lt_u32 %3, 4\n\ts_cbranch_scc0 1f\n\tglobal_load_dword %0, %2, %1\n\ts_waitcnt vmcnt(0)\n1:"
                 : "=&v"(pf), "=&s"(pc) : "v"(min((threadIdx.x & 255u) * 128u, (u32)CODEPF_BYTES)), "s"((u32)(wave - 4) | ((u32)((bid >> 4) - (O0 >> 4)) << 4)) : "scc");
  }
  u32 tstart = now32(); u32 probe = 0;
#define PROBE_AT(k) do { if (PROBE == (k)) probe = now32() - tstart; } while (0)
  const int g = lane >> 4;
  STAMPZ(0);
#define ARGS() \
  u32x16 ar; u32x4 ar2; \
  asm volatile("s_load_dwordx16 %0, %2, 0x0\n\ts_load_dwordx4 %1, %2, 0x40\n\ts_waitcnt lgkmcnt(0)" : "=&s"(ar), "=&s"(ar2) : "s"(__builtin_amdgcn_kernarg_segment_ptr())); \
  const char *kc = mkptr(ar[0], ar[1]), *vc = mkptr(ar[2], ar[3]), *x = mkptr(ar[6], ar[7]), *wq = mkptr(ar[8], ar[9]), *wo = mkptr(ar[10], ar[11]), \
             *wg = mkptr(ar[12], ar[13]), *wu = mkptr(ar[14], ar[15]), *wd = mkptr(ar2[0], ar2[1]); \
  char *ws = (char*)mkptr(ar[4], ar[5]), *out = (char*)mkptr(ar2[2], ar2[3]); \
  const rsrc_t rw = mkrsrc(ws, WS_END);

  if (__builtin_expect(wave < 4, 1)) {
    ARGS()
    PROBE_AT(1);
    const u32 ko = ((lane >> 2) & 3) * 256 + (4 * g + (lane & 3)) * 16;
    const rsrc_t rk = mkrsrc((const char*)kc + (size_t)bid * CHUNK, CHUNK), rv = mkrsrc((const char*)vc + (size_t)bid * CHUNK, CHUNK);
    const bool odd = lane & 1;
    const u32 vo = lane * 16;
    u32x4 r0[8], r1[8], r2[8], r3[8], qg;
    u32 so = wave * 8192, e = 0, qo = 0;
    if (bid < Q0 + NQ) wg_barrier();
#pragma unroll
    for (int p = 0; p < 32; ++p) {
      if (p == NPRO) STAMP2(24);
      if (p == NPRO) {
        u32 ln = lane; asm volatile("" : "+v"(ln));
        qo = WS_QG + (bid >> 3) * 1024 + ((ln >> 1) & 1) * 512 + (ln >> 2) * 32 + (ln & 1) * 16;
      }
      if (p == QPOS) {
        e = __builtin_amdgcn_raw_buffer_load_b32(rw, WS_E, 0, AUX_SC1);
        qg = ldsc(rw, qo, 0);
      }
      if (p >= QPOS && p < QPOS + NPRO) pace<NPRO + 2>();
      else if (p >= NPRO) pace<NPRO>();
      if (p == NPRO) PROBE_AT(2);
      if (p == 8 + NPRO) STAMP2(28);
      if (p == 16 + NPRO) STAMP2(29);
      const u32x4 t = ldnt(rk, ko, so + (p >> 3) * 32768 + (p & 7) * 1024);
      if (p < 8) r0[p] = t; else if (p < 16) r1[p - 8] = t; else if (p < 24) r2[p - 16] = t; else r3[p - 24] = t;
      SBAR();
    }
    PROBE_AT(3);
    const u32 tag = next_tag(__builtin_amdgcn_readfirstlane(e));
    if (!tags_ok<1>(&qg, tag)) {
      u32 spins = SPIN_CAP;
      _Pragma("nounroll") do { RELOAD(); qg = ldsc(rw, qo, 0); } while (!tags_ok<1>(&qg, tag) && --spins);
    }
    PROBE_AT(4);
    Sched sc;
    sc.mask = 0xffu; sc.due = now32() + 0x20000000u; sc.tag = tag;
    sc.lead = (bid >= O0 && bid < F0) ? LEAD_O : ((bid >= F0 && bid < Z0) ? LEAD_F : 0);
    sc.ldo = lane < 8 ? WS_SC + lane * SC_STR : OOB;
    sc.sample = bid == 33 * (bid & 7) && wave == 0;
    sc.sto = lane == 0 ? WS_SC + (bid & 7) * SC_STR : OOB;
    LAS f32x4* sl = (LAS f32x4*)(lds + L_S) + wave * 512 + lane;
    float ml;
    {
      const u32 p0 = dppu<XOR1>(qg[0]), p1 = dppu<XOR1>(qg[2]);
      const u32x4 bq0 = {odd ? 0u : qg[0], odd ? 0u : qg[2], odd ? 0u : p0, odd ? 0u : p1}, bq1 = {odd ? p0 : 0u, odd ? p1 : 0u, odd ? qg[0] : 0u, odd ? qg[2] : 0u};
      u32x4 u = {0u, 0u, 0u, 0u};
      float mx = -INFINITY;
      for (int it = 0; it < 8; ++it) {
        int itv = it; asm volatile("" : "+s"(itv));
        const bool last = itv == 7;
        const rsrc_t r = mkrsrc((last ? vc : kc) + (size_t)bid * CHUNK, CHUNK);
        const u32 lo = last ? vo : ko;
        so = last ? wave * 8192 : so + 131072;
        f32x4 s4;
        s4[0] = k_tile<0>(r0, bq0, bq1, u, r, lo, so, sc, rw, it);
        s4[1] = k_tile<1>(r1, bq0, bq1, u, r, lo, so, sc, rw, it);
        s4[2] = k_tile<2>(r2, bq0, bq1, u, r, lo, so, sc, rw, it);
        s4[3] = k_tile<3>(r3, bq0, bq1, u, r, lo, so, sc, rw, it);
        sl[it * 64] = s4;
        mx = fmaxf(fmaxf(mx, fmaxf(s4[0], s4[1])), fmaxf(s4[2], s4[3]));
        STAMP(8 + it);
      }
      mx = fmaxf(mx, dppf<XOR1>(mx)); mx = fmaxf(mx, dppf<ROR4>(mx)); mx = fmaxf(mx, dppf<ROR8>(mx));
      ml = max32(max16(mx)) * LOG2E;
    }
    float l = 0.f;
    f32x4 acc = {0.f, 0.f, 0.f, 0.f};
    u32x4 bp0 = {0u, 0u, 0u, 0u}, bp1 = {0u, 0u, 0u, 0u};
    const u32 hm = odd ? 0xffff0000u : 0x0000ffffu;
    f32x4 s4 = sl[0];
    for (int it = 8; it < 15; ++it) {
      so += 131072;
      const f32x4 sn = sl[(it - 7) * 64];
      v_tile<0>(r0, probs(s4[0], ml, l, hm), acc, bp0, bp1, rv, vo, so, sc, rw, it);
      v_tile<1>(r1, probs(s4[1], ml, l, hm), acc, bp0, bp1, rv, vo, so, sc, rw, it);
      v_tile<2>(r2, probs(s4[2], ml, l, hm), acc, bp0, bp1, rv, vo, so, sc, rw, it);
      v_tile<3>(r3, probs(s4[3], ml, l, hm), acc, bp0, bp1, rv, vo, so, sc, rw, it);
      s4 = sn;
      STAMP(8 + it);
    }
    const bool isO = bid >= O0 && bid < F0, isF = bid >= F0 && bid < Z0;
    const int u = bid - (isF ? F0 : O0), c = lane & 15;
    const rsrc_t rl = mkrsrc(isF ? wd : wo, isF ? FF * DM * 2 : (isO ? HQ * DH * DM * 2 : 0));
    const u32 wv = (64 * u + 16 * wave + 4 * g) * 1152 + c * 16, wv4 = c < 8 ? wv : OOB;
    u32x4 a[20];
    v_last<0, NE0, 0>(r0, probs(s4[0], ml, l, hm), acc, bp0, bp1, a, rl, wv, wv4);
    v_last<0, 0, 0>(r1, probs(s4[1], ml, l, hm), acc, bp0, bp1, a, rl, wv, wv4);
    v_last<0, 0, 0>(r2, probs(s4[2], ml, l, hm), acc, bp0, bp1, a, rl, wv, wv4);
    const u32x2 z3 = probs(s4[3], ml, l, hm);
    l += dppf<XOR1>(l); l += dppf<ROR4>(l); l += dppf<ROR8>(l); l = sum32(sum16(l));
    LAS float* lf = (LAS float*)lds;
    if (lane < 4) lf[L_ML + lane * 4 + wave] = odd ? l : ml;
    lds[L_TAG] = tag;
    PROBE_AT(5);
    if (wave == 0) lds[L_TAG + 1] = probe;
    u32 ad = (L_ACC + wave * ACC_W + ((lane >> 1) & 1) * 176 + g * 40 + ((lane >> 2) & 3) * 2 + (lane & 1)) * 4;
    u32 as = (L_ACC + (lane >> 5) * 176 + ((lane >> 3) & 3) * 40 + (lane & 7) * 4) * 4, am = (L_ML + (lane >> 5) * 8) * 4;
    u32 ao = WS_AG + (((bid >> 3) & 15) * 16 + (lane >> 2)) * 1024 + (((bid >> 1) & 3) * 4 + (bid >> 7) * 2 + (bid & 1)) * 64 + (lane & 3) * 16;
    asm volatile("" : "+v"(ao), "+v"(ad), "+v"(as), "+v"(am));
    SBAR();
    v_last<3, 5, NE0>(r3, z3, acc, bp0, bp1, a, rl, wv, wv4);
    STAMP2(3);
#pragma unroll
    for (int i = 0; i < 4; ++i) *(LAS float*)((LAS char*)lds + ad + 32 * i) = acc[i];
    wg_barrier();
    STAMP(4);
    if (wave == 0) {
      const f32x4 mw = *(const LAS f32x4*)((LAS char*)lds + am), lw = *(const LAS f32x4*)((LAS char*)lds + am + 16);
      f32x4 pa[4];
#pragma unroll
      for (int w = 0; w < 4; ++w) pa[w] = *(const LAS f32x4*)((LAS char*)lds + as + w * (ACC_W * 4));
      SBAR();
      const float M = fmaxf(fmaxf(mw[0], mw[1]), fmaxf(mw[2], mw[3]));
      const f32x4 ew = {ex2(mw[0] - M), ex2(mw[1] - M), ex2(mw[2] - M), ex2(mw[3] - M)};
      const float L = fmaf(ew[0], lw[0], ew[1] * lw[1]) + fmaf(ew[2], lw[2], ew[3] * lw[3]);
      const f32x4 o = (ew[0] * pa[0] + ew[1] * pa[1] + ew[2] * pa[2] + ew[3] * pa[3]) * rcp(L);
      const u32 lse = asu(M + __builtin_amdgcn_logf(L));
      stsc(rw, ao, u32x4{pk(o[0], o[1]), __builtin_amdgcn_perm(tag, lse, 0x05040100u), pk(o[2], o[3]), __builtin_amdgcn_perm(tag, lse, 0x05040302u)});
    }
    STAMP2(5);
    if (isO || isF) {
      rows_load<NE0 + 5, 20>(a, rl, wv, wv4);
      Sel sel = make_sel(lane);
      u32 po = (isF ? WS_FG : WS_YG) + ((wave * 8 + (lane >> 3)) * NF + u) * 128 + (lane & 7) * 16, po2 = (wave == 0 && lane < 32) ? po + 32 * NF * 128 : OOB;
      u32 yd = L_Y * 4 + (wave * 400 + g * 20 + c) * 16, ys = L_Y * 4 + (wave * 80 + (lane & 3) * 20 + (lane >> 2)) * 16;
      pin_sel(sel);
      asm volatile("" : "+v"(po), "+v"(po2), "+v"(yd), "+v"(ys));
      STAMP(6);
      u32 m0, m1;
      if (isO) {
        const u32 go = WS_AG + (u * 4 + wave) * 1024 + c * 64 + g * 16;
        if (DLY_O) __builtin_amdgcn_s_sleep(DLY_O);
        u32x4 r;
        _Pragma("nounroll") for (u32 spins = SPIN_CAP; spins; --spins) {
          RELOAD();
          r = ldsc(rw, go, 0);
          if (tags16_ok<1>(&r, tag)) break;
        }
        STAMP2(7);
        const float lse = asf(__builtin_amdgcn_perm(r[3], r[1], 0x05040100u));
        float M = fmaxf(lse, dppf<XOR1>(lse)); M = fmaxf(M, dppf<ROR4>(M)); M = fmaxf(M, dppf<ROR8>(M));
        const float wt = ex2(lse - M);
        float o[5] = {wt * lo16(r[0]), wt * hi16(r[0]), wt * lo16(r[2]), wt * hi16(r[2]), wt};
#define RED5(C) _Pragma("unroll") for (int i = 0; i < 5; ++i) { o[i] += dppf<C>(o[i]); asm volatile("" : "+v"(o[i])); }
        asm volatile("" : "+v"(o[0]), "+v"(o[1]), "+v"(o[2]), "+v"(o[3]));
        RED5(XOR1) RED5(ROR4) RED5(ROR8)
#undef RED5
        const float inv = rcp(o[4]);
        m0 = pk(o[0] * inv, o[1] * inv); m1 = pk(o[2] * inv, o[3] * inv);
      } else {
        const LAS float* red = lf + L_RED;
        const int cc = 2 * wave + (g >> 1);
        const int src = ((lane >> 2) & 3) * 256 + (cc & 3) * 64 + ((lane & 1) * 2 + (cc >> 2)) * 16 + (g & 1) * 8 + (lane & 2);
        const f32x2 h0 = *(const LAS f32x2*)(red + src), h1 = *(const LAS f32x2*)(red + src + 4);
        float h[4] = {h0[0], h0[1], h1[0], h1[1]};
#pragma unroll
        for (int i = 0; i < 4; ++i) {
          h[i] += dppf<ROR4>(h[i]); h[i] += dppf<ROR8>(h[i]);
          const float own = rbf(h[i]), oth = dppf<XOR1>(own);
          const float gt = odd ? oth : own, up = odd ? own : oth;
          h[i] = rbf(silu(gt)) * up;
        }
        m0 = pk(h[0], h[1]); m1 = pk(h[2], h[3]);
      }
      f32x4 y[5];
      const u32x4 b0 = build_b(m0, sel, 0), b1 = build_b(m0, sel, 1), b2 = build_b(m1, sel, 0), b3 = build_b(m1, sel, 1);
#pragma unroll
      for (int cb = 0; cb < 5; ++cb) y[cb] = mfma(b0, a[cb], f32x4{0.f, 0.f, 0.f, 0.f});
#pragma unroll
      for (int cb = 0; cb < 5; ++cb) y[cb] = mfma(b1, a[5 + cb], y[cb]);
      SBAR();
#pragma unroll
      for (int cb = 0; cb < 5; ++cb) y[cb] = mfma(b2, a[10 + cb], y[cb]);
      SBAR();
#pragma unroll
      for (int cb = 0; cb < 5; ++cb) { y[cb] = mfma(b3, a[15 + cb], y[cb]); SBAR(); }
#pragma unroll
      for (int cb = 0; cb < 5; ++cb) *(LAS f32x4*)((LAS char*)lds + yd + cb * 1280) = y[cb];
      wg_barrier();
      const LAS f32x4* p = (const LAS f32x4*)((LAS char*)lds + ys);
      const f32x4 t0 = p[0], t1 = p[400], t2 = p[800], t3 = p[1200], t4 = p[320], t5 = p[720], t6 = p[1120], t7 = p[1520];
      stsc(rw, po, pack4((t0 + t1) + (t2 + t3), tag));
      stsc(rw, po2, pack4((t4 + t5) + (t6 + t7), tag));
      STAMP(30);
    }
    STAMP_OUT();
    return;
  }

  const int aw = wave - 4, c = lane & 15;
  if (__builtin_expect(bid < Q0 + NQ, 1)) {
    u32 vo = (144 * aw + 36 * g + (c >> 3)) * 8192 + bid * 128 + (c & 7) * 16, xo = ((lane >> 5) * 288 + (lane & 31) * 3) * 4;
    asm volatile("" : "+v"(vo), "+v"(xo));
    ARGS()
    const rsrc_t rx = mkrsrc(x, 2 * DM * 2), rq = mkrsrc(wq, DM * HQ * DH * 2);
    u32 xd[9]; u32x4 a[18];
    load_x(rx, xo, xd);
#pragma unroll
    for (int k = 0; k < QFIRST; ++k) a[k] = ldnt(rq, vo, k * 16384);
    SBAR();
    STAMP2(24);
    if (aw == 0 && lane == 0) lds[L_CTR] = 0u;
    const Sel sel = make_sel(lane);
    u32 xm[18];
    layernorm(xd, lds + L_XN + aw * 576, lane, aw, xm);
    STAMP2(1);
    SBAR();
    f32x4 d0 = {0.f, 0.f, 0.f, 0.f}, d1 = d0;
    u32 e = 0;
#pragma unroll
    for (int k = 0; k < 18; ++k) {
      if (k + QFIRST < 18) a[k + QFIRST] = ldnt(rq, vo, (k + QFIRST) * 16384);
      if (k + QFIRST == 18) { e = __builtin_amdgcn_raw_buffer_load_b32(rw, WS_E, 0, AUX_SC1); SBAR(); wg_barrier(); STAMP2(25); }
      SBAR();
      d0 = mfma(a[k], build_b(xm[k], sel, 0), d0); d1 = mfma(a[k], build_b(xm[k], sel, 1), d1);
      SBAR();
    }
    LAS float* red = (LAS float*)lds + L_RED;
#pragma unroll
    for (int i = 0; i < 4; ++i) red[(aw * 4 + i) * 64 + lane] = lane < 32 ? d0[i] : d1[i];
    lds_fence();
    STAMP2(2);
    u32 ticket = 0;
    if (lane == 0) ticket = __hip_atomic_fetch_add(lds + L_CTR, 1u, __ATOMIC_RELAXED, __HIP_MEMORY_SCOPE_WORKGROUP);
    if (__builtin_amdgcn_readfirstlane(ticket) == 3u) {
      asm volatile("" ::: "memory");
      const u32 tag = next_tag(__builtin_amdgcn_readfirstlane(e));
      const int bt = lane >> 5, cp = lane & 31, cc = cp >> 2;
      const int src = (cc & 3) * 64 + (cc >> 2) * 16 + (cp & 3) * 4 + 2 * bt;
      f32x2 s = {0.f, 0.f};
#pragma unroll
      for (int w = 0; w < 4; ++w) s += *(const LAS f32x2*)(red + w * 256 + src) + *(const LAS f32x2*)(red + w * 256 + src + 32);
      __builtin_amdgcn_raw_buffer_store_b64(u32x2{pk(s[0], s[1]), tag}, rw, WS_QG + (bt * 2048 + bid * 32 + cp) * 8, 0, AUX_SC1);
    }
    STAMP(6);
    if (aw >= 2) { STAMP_OUT(); return; }
  }
  ARGS()
  if (bid >= F0 && bid < F0 + NF) {
    const int u = bid - F0;
    const rsrc_t rx = mkrsrc(x, 2 * DM * 2);
    u32 xd[9];
    load_x(rx, ((lane >> 5) * 288 + (lane & 31) * 3) * 4, xd);
    const GAS u32x4* gp = (const GAS u32x4*)((const GAS char*)(c < 8 ? wg : wu) + (size_t)(144 * aw + 36 * g) * (FF * 2) + u * 128 + (c & 7) * 16);
    u32x4 ring[NAUX];
#pragma unroll
    for (int k = 0; k < NAUX; ++k) ring[k] = __builtin_nontemporal_load(gp + k * (FF * 2 / 16));
    SBAR();
    const Sel sel = make_sel(lane);
    u32 xm[18];
    layernorm(xd, lds + L_XN + aw * 576, lane, aw, xm);
    SBAR();
    f32x4 d = {0.f, 0.f, 0.f, 0.f};
#pragma unroll
    for (int k = 0; k < 36; ++k) {
      d = mfma(ring[k % NAUX], build_b(xm[k >> 1], sel, k & 1), d);
      SBAR();
      if (k + NAUX < 36) ring[k % NAUX] = __builtin_nontemporal_load(gp + (k + NAUX) * (FF * 2 / 16));
      SBAR();
    }
    LAS float* red = (LAS float*)lds + L_RED;
#pragma unroll
    for (int i = 0; i < 4; ++i) red[(aw * 4 + i) * 64 + lane] = d[i];
    STAMP(1);
    STAMP_OUT();
    wg_barrier();
    return;
  }
  if ((bid < NQ || (bid >= Z0 && bid < Z0 + 8)) && aw < 2) {
    const int f = bid < NQ ? bid : bid - (Z0 - NQ);
    const u32 oi = (lane >> 5) * DM + 8 * f + 2 * (lane & 3) + (g & 1);
    u32 xv = ((const GAS u16*)x)[oi];
    GAS u16* op = (GAS u16*)out + oi;
    u32 go = (aw ? WS_FG : WS_YG) + (f >> 1) * (NF * 128) + (lane >> 2) * 128 + (f & 1) * 64 + (lane & 3) * 16;
    asm volatile("" : "+v"(xv), "+v"(op), "+v"(go));
    wg_barrier();
    STAMPZ(31);
    const u32 tag = lds[L_TAG];
    LAS float* lg = (LAS float*)lds + L_RED + lane;
    u32x4 r[5];
    if (aw) {
      if (DLY_ZF) __builtin_amdgcn_s_sleep(DLY_ZF);
      _Pragma("nounroll") for (u32 spins = SPIN_CAP; spins; --spins) {
        RELOAD();
#pragma unroll
        for (int k = 0; k < 5; ++k) r[k] = ldsc(rw, (k == 4 && lane >= 32) ? OOB : go, k * 2048);
        if (lane >= 32) { r[4][1] = tag << 16; r[4][3] = tag << 16; }
        if (tags16_ok<5>(r, tag)) break;
      }
      *lg = rbf(silu(rbf(sum_parts<5>(r))));
      STAMPZ(7);
      wg_barrier();
      STAMPZ_OUT();
      return;
    }
    if (DLY_ZY) __builtin_amdgcn_s_sleep(DLY_ZY);
    _Pragma("nounroll") for (u32 spins = SPIN_CAP; spins; --spins) {
      RELOAD();
#pragma unroll
      for (int k = 0; k < 4; ++k) r[k] = ldsc(rw, go, k * 2048);
      if (tags16_ok<4>(r, tag)) break;
    }
    STAMP2(3);
    wg_barrier();
    float gt = *lg;
    SBAR();
    float sm = rbf(sum_parts<4>(r));
    asm volatile("" : "+v"(gt), "+v"(sm));
    const u32 res = pk(lo16(xv) + rbf(sm * gt), 0.f);
    if ((lane & 12) == 0) *op = (u16)res;
    if (f == 0 && lane == 0) __builtin_amdgcn_raw_buffer_store_b32(tag, rw, WS_E, 0, AUX_SC1);
    if (f == 64) {
      probe = __builtin_amdgcn_readfirstlane(lds[L_TAG + 1]);
      PROBE_AT(6);
      while (now32() - tstart < 20000u + (PROBE < 5 ? 50u : 10u) * probe) __builtin_amdgcn_s_sleep(1);
    }
    return;
  }
}

extern "C" void kernel_launch(void* const* d_in, const int* in_sizes, int n_in, void* d_out, int out_size, void* d_ws, size_t ws_size,
                              hipStream_t stream) {
  fused_decode<<<NWG, NTHR, 0, stream>>>((const u16*)d_in[1], (const u16*)d_in[2], (unsigned char*)d_ws, (const u16*)d_in[0], (const u16*)d_in[3],
                                          (const u16*)d_in[4], (const u16*)d_in[6], (const u16*)d_in[7], (const u16*)d_in[8], (u16*)d_out);
}
```

```cpp
#include <hip/hip_runtime.h>
#include <hip/hip_bf16.h>
#include <stdint.h>

typedef unsigned u32;
typedef unsigned short u16;
typedef u32 u32x2 __attribute__((ext_vector_type(2)));
typedef u32 u32x3 __attribute__((ext_vector_type(3)));
typedef u32 u32x4 __attribute__((ext_vector_type(4)));
typedef float f32x2 __attribute__((ext_vector_type(2)));
typedef float f32x4 __attribute__((ext_vector_type(4)));
typedef __bf16 bf16x8 __attribute__((ext_vector_type(8)));
typedef __bf16 bf16x2 __attribute__((ext_vector_type(2)));
typedef __amdgpu_buffer_rsrc_t rsrc_t;
#define GAS __attribute__((address_space(1)))
#define LAS __attribute__((address_space(3)))

#define NPRO 5
#define NFL 5
#define NHI 9
#define SCH_DT 219
#define SCH_SH 5
#define SCH_BIAS 48
#define SCH_IT0 2
#define SCH_TAB 0x0001114955FFull
#define LEAD_O 30
#define LEAD_F 30
#define QPOS 26
#define NAUX 9
#define OUT_AUX 16
#define PF0 208
#define NE1 5
#define NPRE 1
#define IDLE 48
#define RELEASE 0
#define NE0 3
#define NLATE 8
#define CODEPF_BYTES 15360
#define CODEPF 1

constexpr int DM = 576, HQ = 32, SK = 32768, DH = 128, FF = 4608;
constexpr int NWG = 256, NTHR = 512;
constexpr u32 CHUNK = 4096u * DH * 2u;
constexpr int TPW = 32;
constexpr int AUX_NT = 2, AUX_SC1 = 16;
constexpr u32 OOB = 0x80000000u;
constexpr int NU = 208, O0 = 64, F0 = 128, NF = 72, Z0 = 200;
constexpr u32 WS_E = 0, WS_PG = 4096, WS_AG = WS_PG + NU * 1024, WS_YG = WS_AG + NWG * 1024, WS_FG = WS_YG + 36 * NF * 128,
              WS_SC = WS_FG + 36 * NF * 128, SC_STR = 4352, WS_END = WS_SC + 16 * 8 * SC_STR;
constexpr u32 SPIN_CAP = 400000;
constexpr int L_CTR = 0, L_TAG = 4, L_ML = 8, L_ACC = 32, ACC_W = 352, L_XN = L_ACC + 4 * ACC_W, L_RED = L_XN + 4 * 576, L_Y = L_RED + 4 * ACC_W,
              L_S = L_Y + 4 * 1600, L_END = L_S + 4 * 2048;
constexpr float LOG2E = 1.4426950408889634f;

#define STAMPZ(k)
#define STAMPZ_OUT()
#define STAMP(k)
#define STAMP_OUT()
#define STAMP2(k)

__device__ __forceinline__ rsrc_t mkrsrc(const void* p, u32 bytes) { return __builtin_amdgcn_make_buffer_rsrc((void*)p, 0, bytes, 0x00020000); }
__device__ __forceinline__ u32x4 ldnt(rsrc_t r, u32 vo, u32 so) { return __builtin_amdgcn_raw_buffer_load_b128(r, vo, so, AUX_NT); }
__device__ __forceinline__ u32x4 ldsc(rsrc_t r, u32 vo, u32 so) { return __builtin_amdgcn_raw_buffer_load_b128(r, vo, so, AUX_SC1); }
__device__ __forceinline__ void stsc(rsrc_t r, u32 vo, u32x4 v) { __builtin_amdgcn_raw_buffer_store_b128(v, r, vo, 0, AUX_SC1); }
__device__ __forceinline__ f32x4 mfma(u32x4 a, u32x4 b, f32x4 c) {
  return __builtin_amdgcn_mfma_f32_16x16x32_bf16(__builtin_bit_cast(bf16x8, a), __builtin_bit_cast(bf16x8, b), c, 0, 0, 0);
}
__device__ __forceinline__ float asf(u32 x) { return __builtin_bit_cast(float, x); }
__device__ __forceinline__ u32 asu(float x) { return __builtin_bit_cast(u32, x); }
__device__ __forceinline__ u32 pk(float lo, float hi) { bf16x2 v = {(__bf16)lo, (__bf16)hi}; return __builtin_bit_cast(u32, v); }
__device__ __forceinline__ float rbf(float x) { return (float)(__bf16)x; }
__device__ __forceinline__ float lo16(u32 x) { return asf(x << 16); }
__device__ __forceinline__ float hi16(u32 x) { return asf(x & 0xffff0000u); }
template <int CTRL> __device__ __forceinline__ u32 dppu(u32 x) { return (u32)__builtin_amdgcn_mov_dpp((int)x, CTRL, 0xf, 0xf, true); }
template <int CTRL> __device__ __forceinline__ float dppf(float x) { return asf(dppu<CTRL>(asu(x))); }
constexpr int XOR1 = 0xB1, XOR2 = 0x4E, XOR7 = 0x141, ROR4 = 0x124, ROR8 = 0x128;
__device__ __forceinline__ float sum16(float x) { auto s = __builtin_amdgcn_permlane16_swap(asu(x), asu(x), false, false); return asf(s[0]) + asf(s[1]); }
__device__ __forceinline__ float sum32(float x) { auto s = __builtin_amdgcn_permlane32_swap(asu(x), asu(x), false, false); return asf(s[0]) + asf(s[1]); }
__device__ __forceinline__ float max16(float x) { auto s = __builtin_amdgcn_permlane16_swap(asu(x), asu(x), false, false); return fmaxf(asf(s[0]), asf(s[1])); }
__device__ __forceinline__ float max32(float x) { auto s = __builtin_amdgcn_permlane32_swap(asu(x), asu(x), false, false); return fmaxf(asf(s[0]), asf(s[1])); }
__device__ __forceinline__ float ex2(float x) { return __builtin_amdgcn_exp2f(x); }
__device__ __forceinline__ float rcp(float x) { return __builtin_amdgcn_rcpf(x); }
__device__ __forceinline__ float silu(float x) { return x * rcp(1.0f + ex2(-LOG2E * x)); }
__device__ __forceinline__ u32 now32() { return (u32)__builtin_amdgcn_s_memrealtime(); }
template <int N> __device__ __forceinline__ void pace() { asm volatile("s_waitcnt vmcnt(%0)" ::"n"(N - 1) : "memory"); }
template <int BIT, int EXTRA> __device__ __forceinline__ void pacev(u32 mask) {
  asm volatile("s_waitcnt vmcnt(%1)\n\ts_bitcmp1_b32 %0, %3\n\ts_cbranch_scc0 1f\n\ts_waitcnt vmcnt(%2)\n1:" ::"s"(mask), "n"(NHI - 1 + EXTRA), "n"(NFL - 1 + EXTRA), "n"(BIT) : "memory", "scc");
}
__device__ __forceinline__ u32 pace_mask(u32 now, u32 due) {
  int lev = (int)(now - due) >> SCH_SH;
  asm("s_max_i32 %0, %0, 0\n\ts_min_i32 %0, %0, 7" : "+s"(lev) : : "scc");
  return (u32)(SCH_TAB >> (8 * lev));
}
#define SBAR() __builtin_amdgcn_sched_barrier(0)
#define RELOAD() asm volatile("" ::: "memory")
__device__ __forceinline__ void lds_fence() { asm volatile("s_waitcnt lgkmcnt(0)" ::: "memory"); }
__device__ __forceinline__ void wg_barrier() { lds_fence(); __builtin_amdgcn_s_barrier(); asm volatile("" ::: "memory"); }
__device__ __forceinline__ u32 next_tag(u32 e) { return e * 1664525u + 1013904223u; }

struct Sched { u32 mask, due, tag, lead, ldo, sto; bool sample; u32x2 row; };

template <int W, int T> __device__ __forceinline__ u32 bank_mov(u32 old, u32 z) {
  constexpr int ctrl = W == T ? 0xE4 : (W > T ? 0x110 + 4 * (W - T) : 0x100 + 4 * (T - W));
  return (u32)__builtin_amdgcn_update_dpp((int)old, (int)z, ctrl, 0xf, 1 << W, false);
}
template <int T> __device__ __forceinline__ void build_p(u32x4& b, u32 z) {
  b[0] = bank_mov<0, T>(b[0], z); b[1] = bank_mov<1, T>(b[1], z); b[2] = bank_mov<2, T>(b[2], z); b[3] = bank_mov<3, T>(b[3], z);
}

template <int GRP> __device__ __forceinline__ void sched_step(Sched& sc, u32 now, rsrc_t rw, int it) {
  if (GRP == 0) {
    sc.row = __builtin_amdgcn_raw_buffer_load_b64(rw, sc.ldo, it * (8 * SC_STR), AUX_SC1);
    if (sc.sample) __builtin_amdgcn_raw_buffer_store_b64(u32x2{now + sc.lead, sc.tag}, rw, sc.sto, (it + 1) * (8 * SC_STR), AUX_SC1);
  } else {
    const bool ok = __builtin_amdgcn_ballot_w64(sc.row[1] == sc.tag) == 0xffull;
    u32 d = sc.row[0] - now;
    d += dppu<XOR1>(d); d += dppu<XOR2>(d); d += dppu<XOR7>(d);
    const u32 due = now + ((int)__builtin_amdgcn_readfirstlane(d) >> 3) + (3 * SCH_DT - SCH_BIAS) - sc.lead;
    if (ok && it >= SCH_IT0) sc.due = due;
  }
  sc.mask = pace_mask(now, sc.due);
  sc.due += SCH_DT;
}
template <int GRP, int I>
__device__ __forceinline__ void refill(u32x4& slot, rsrc_t r, u32 vo, u32 so, Sched& sc, u32& now, rsrc_t rw, int it) {
  pacev<I, (GRP == 0 && I >= 2 && I < 2 + NFL) ? 1 : 0>(sc.mask);
  slot = ldnt(r, vo, so + GRP * 32768 + I * 1024);
  if (!(GRP & 1) && I == 0) now = now32();
  if (!(GRP & 1) && I == 1) sched_step<GRP>(sc, now, rw, it);
  SBAR();
}
template <int T> __device__ __forceinline__ void k_pick(u32x4& u, f32x4 d) {
  u[0] = bank_mov<T, 0>(u[0], asu(d[0])); u[1] = bank_mov<T, 1>(u[1], asu(d[1])); u[2] = bank_mov<T, 2>(u[2], asu(d[2])); u[3] = bank_mov<T, 3>(u[3], asu(d[3]));
}
template <int GRP>
__device__ __forceinline__ float k_tile(u32x4 (&k)[8], u32x4 bq0, u32x4 bq1, u32x4& u, rsrc_t r, u32 vo, u32 so, Sched& sc, rsrc_t rw, int it) {
  const f32x4 z = {0.f, 0.f, 0.f, 0.f};
  f32x4 da, db;
  u32 now = 0;
#define RF(I) SBAR(); refill<GRP, I>(k[I], r, vo, so, sc, now, rw, it);
  da = mfma(k[0], bq0, z);                     RF(0)
  da = mfma(k[1], bq1, da);                    RF(1)
  db = mfma(k[2], bq0, z);                     RF(2)
  db = mfma(k[3], bq1, db); k_pick<0>(u, da);  RF(3)
  da = mfma(k[4], bq0, z);                     RF(4)
  da = mfma(k[5], bq1, da); k_pick<1>(u, db);  RF(5)
  db = mfma(k[6], bq0, z);                     RF(6)
  db = mfma(k[7], bq1, db); k_pick<2>(u, da);  RF(7)
#undef RF
  k_pick<3>(u, db);
  const float s = (asf(u[0]) + asf(u[1])) + (asf(u[2]) + asf(u[3]));
  return rbf(rbf(s) * 0.08838834764831845f);
}
__device__ __forceinline__ u32x2 probs(float s, float ml, float& l, u32 hm) {
  const float p = ex2(fmaf(s, LOG2E, -ml));
  l += p;
  const u32 t = pk(p, p);
  return u32x2{dppu<0xA0>(t) & hm, dppu<0xF5>(t) & hm};
}
template <int GRP>
__device__ __forceinline__ void v_tile(u32x4 (&v)[8], u32x2 z, f32x4& acc, u32x4& bp0, u32x4& bp1, rsrc_t r, u32 vo, u32 so, Sched& sc, rsrc_t rw, int it) {
  u32 now = 0;
#define PV_STEP(U, BP) build_p<(U) / 2>(BP, z[(U) & 1]); acc = mfma(v[U], BP, acc); SBAR(); refill<GRP, U>(v[U], r, vo, so, sc, now, rw, it);
  PV_STEP(0, bp0) PV_STEP(1, bp1) PV_STEP(2, bp0) PV_STEP(3, bp1) PV_STEP(4, bp0) PV_STEP(5, bp1) PV_STEP(6, bp0) PV_STEP(7, bp1)
#undef PV_STEP
}
template <int K> __device__ __forceinline__ u32x4 row_load(rsrc_t r, u32 vo, u32 vo4) { return ldnt(r, K % 5 == 4 ? vo4 : vo, (K / 5) * 1152 + (K % 5) * 256); }
template <int FROM, int CNT, int K0>
__device__ __forceinline__ void v_last(const u32x4 (&v)[8], u32x2 z, f32x4& acc, u32x4& bp0, u32x4& bp1, u32x4 (&a)[20], rsrc_t r, u32 vo, u32 vo4) {
#define PV_STEP(U, BP) build_p<(U) / 2>(BP, z[(U) & 1]); acc = mfma(v[U], BP, acc); SBAR(); \
  if constexpr (U >= FROM && U < FROM + CNT) { a[K0 + U - FROM] = row_load<K0 + U - FROM>(r, vo, vo4); SBAR(); }
  PV_STEP(0, bp0) PV_STEP(1, bp1) PV_STEP(2, bp0) PV_STEP(3, bp1) PV_STEP(4, bp0) PV_STEP(5, bp1) PV_STEP(6, bp0) PV_STEP(7, bp1)
#undef PV_STEP
}
template <int LO, int HI> __device__ __forceinline__ void rows_load(u32x4 (&a)[20], rsrc_t r, u32 vo, u32 vo4) {
  if constexpr (LO < HI) { pace<NLATE>(); a[LO] = row_load<LO>(r, vo, vo4); SBAR(); rows_load<LO + 1, HI>(a, r, vo, vo4); }
}

struct Sel { u32 s[4][2]; };
__device__ __forceinline__ Sel make_sel(int lane) {
  Sel r; const int wp = (lane >> 2) & 3; const bool hb = lane & 1;
#pragma unroll
  for (int par = 0; par < 2; ++par) {
    const u32 two = ((2 * par + 1) << 8) | (2 * par);
    const u32 base = hb ? ((two << 16) | 0x0c0cu) : (0x0c0c0000u | two);
#pragma unroll
    for (int w = 0; w < 4; ++w) r.s[w][par] = wp == w ? base : 0x0c0c0c0cu;
  }
  return r;
}
__device__ __forceinline__ void pin_sel(Sel& s) {
  asm volatile("" : "+v"(s.s[0][0]), "+v"(s.s[0][1]), "+v"(s.s[1][0]), "+v"(s.s[1][1]), "+v"(s.s[2][0]), "+v"(s.s[2][1]), "+v"(s.s[3][0]), "+v"(s.s[3][1]));
}
__device__ __forceinline__ u32x4 build_b(u32 d, const Sel& sel, int par) {
  u32x4 b;
#pragma unroll
  for (int w = 0; w < 4; ++w) b[w] = __builtin_amdgcn_perm(d, d, sel.s[w][par]);
  return b;
}
__device__ __forceinline__ void load_x(rsrc_t rx, u32 xo, u32x3 (&xr)[3]) {
#pragma unroll
  for (int k = 0; k < 3; ++k) xr[k] = __builtin_amdgcn_raw_buffer_load_b96(rx, xo, k * 384, 0);
}
template <int LO, int HI, int N> __device__ __forceinline__ void unit_load(u32x4 (&a)[18], const GAS char*& gp, u32 stride) {
  if constexpr (LO < HI) {
    if constexpr (N > 0) pace<N>();
    a[LO] = __builtin_nontemporal_load((const GAS u32x4*)gp);
    gp += stride;
    if constexpr (LO == 17 - RELEASE) __builtin_amdgcn_s_barrier();
    SBAR();
    unit_load<LO + 1, HI, N>(a, gp, stride);
  }
}
__device__ __forceinline__ void layernorm(const u32x3 (&xr)[3], LAS u32* xn, int lane) {
  f32x2 v[9], s2 = {0.f, 0.f};
#pragma unroll
  for (int k = 0; k < 9; ++k) { v[k] = f32x2{lo16(xr[k / 3][k % 3]), hi16(xr[k / 3][k % 3])}; s2 += v[k]; }
  float s = s2[0] + s2[1];
  s += dppf<XOR1>(s); s += dppf<XOR2>(s); s += dppf<ROR4>(s); s += dppf<ROR8>(s); s = sum16(s);
  const float mean = s * (1.0f / DM);
  f32x2 q2 = {0.f, 0.f};
#pragma unroll
  for (int k = 0; k < 9; ++k) { const f32x2 a = v[k] - mean; q2 += a * a; }
  float q = q2[0] + q2[1];
  q += dppf<XOR1>(q); q += dppf<XOR2>(q); q += dppf<ROR4>(q); q += dppf<ROR8>(q); q = sum16(q);
  const float mb = rbf(mean), den = rbf(__builtin_amdgcn_sqrtf(rbf(rbf(q * (1.0f / DM)) + 1e-5f))), inv = rcp(den);
#pragma unroll
  for (int k = 0; k < 9; ++k) {
    const f32x2 a = v[k] - mb;
    const u32 r = pk(a[0], a[1]);
    const f32x2 b = f32x2{lo16(r), hi16(r)} * inv;
    xn[(lane >> 5) * 288 + (lane & 31) * 3 + 96 * (k / 3) + k % 3] = pk(b[0], b[1]);
  }
  lds_fence();
}
__device__ __forceinline__ u32x4 pack4(f32x4 v, u32 tag) {
  return u32x4{__builtin_amdgcn_perm(asu(v[1]), asu(v[0]), 0x05030201u), __builtin_amdgcn_perm(tag, asu(v[1]), 0x05040302u),
               __builtin_amdgcn_perm(asu(v[3]), asu(v[2]), 0x05030201u), __builtin_amdgcn_perm(tag, asu(v[3]), 0x05040302u)};
}
__device__ __forceinline__ f32x4 unpack4(u32x4 r) {
  return f32x4{asf(r[0] << 8), asf(__builtin_amdgcn_perm(r[1], r[0], 0x0504030cu)), asf(r[2] << 8), asf(__builtin_amdgcn_perm(r[3], r[2], 0x0504030cu))};
}
template <int N> __device__ __forceinline__ bool tags16_ok(const u32x4* r, u32 tag) {
  u32 bad = 0;
#pragma unroll
  for (int i = 0; i < N; ++i) bad |= (r[i][1] ^ (tag << 16)) | (r[i][3] ^ (tag << 16));
  return __builtin_amdgcn_ballot_w64(bad > 0xffffu) == 0ull;
}
typedef unsigned long long u64;
#define G_LD(M, R, SO, OFF) "s_wqm_b64 exec, " M "\n\tbuffer_load_dwordx4 " R ", %[vo], %[rs], " SO " offen " OFF " sc1\n\t"
#define G_CK(M, A, B) "v_bitop3_b32 %[t], " A ", " B ", %[tg] bitop3:0x7e\n\tv_cmp_lt_u32_e64 " M ", %[lim], %[t]\n\t"
#define G_END "s_cbranch_scc0 2f\n\ts_sub_u32 %[n], %[n], 1\n\ts_cbranch_scc0 1b\n2:"
__device__ __forceinline__ u32x4 gather1(u32x4 rs, u32 vo, u32 t16) {
  u32x4 r; u64 m0; u32 t, n = SPIN_CAP;
  asm volatile("1:\n\tbuffer_load_dwordx4 v[140:143], %[vo], %[rs], 0 offen sc1\n\ts_waitcnt vmcnt(0)\n\t" G_CK("%[m0]", "v141", "v143")
               "s_cmp_lg_u64 %[m0], 0\n\t" G_END
               : "=&{v[140:143]}"(r), [m0] "=&s"(m0), [t] "=&v"(t), [n] "+s"(n) : [vo] "v"(vo), [rs] "s"(rs), [tg] "s"(t16), [lim] "v"(0xffffu) : "memory", "scc");
  return r;
}
template <int N> __device__ __forceinline__ void gather(u32x4 (&r)[5], u32x4 rs, u32 vo, u32 t16) {
  u64 m0 = ~0ull, m1 = ~0ull, m2 = ~0ull, m3 = ~0ull, m4 = N > 4 ? 0xffffffffull : 0ull, any; u32 t, n = SPIN_CAP;
  r[4] = u32x4{0u, t16, 0u, t16};
  asm volatile("1:\n\t" G_LD("%[m0]", "v[40:43]", "0", "") G_LD("%[m1]", "v[44:47]", "0", "offset:2048") G_LD("%[m2]", "v[48:51]", "%[s4]", "")
               G_LD("%[m3]", "v[52:55]", "%[s4]", "offset:2048") ".if %[five]\n\t" G_LD("%[m4]", "v[56:59]", "%[s8]", "") ".endif\n\t"
               "s_mov_b64 exec, -1\n\ts_waitcnt vmcnt(0)\n\t"
               G_CK("%[m0]", "v41", "v43") G_CK("%[m1]", "v45", "v47") G_CK("%[m2]", "v49", "v51") G_CK("%[m3]", "v53", "v55")
               ".if %[five]\n\t" G_CK("%[m4]", "v57", "v59") ".endif\n\t"
               "s_or_b64 %[any], %[m0], %[m1]\n\ts_or_b64 %[any], %[any], %[m2]\n\ts_or_b64 %[any], %[any], %[m3]\n\t"
               ".if %[five]\n\ts_or_b64 %[any], %[any], %[m4]\n\t.endif\n\t" G_END
               : "=&{v[40:43]}"(r[0]), "=&{v[44:47]}"(r[1]), "=&{v[48:51]}"(r[2]), "=&{v[52:55]}"(r[3]), "+{v[56:59]}"(r[4]), [m0] "+s"(m0), [m1] "+s"(m1),
                 [m2] "+s"(m2), [m3] "+s"(m3), [m4] "+s"(m4), [any] "=&s"(any), [t] "=&v"(t), [n] "+s"(n)
               : [vo] "v"(vo), [rs] "s"(rs), [tg] "s"(t16), [lim] "v"(0xffffu), [s4] "s"(4096u), [s8] "s"(8192u), [five] "n"(N > 4) : "memory", "scc");
}
template <int N> __device__ __forceinline__ float sum_parts(const u32x4* r) {
  f32x4 a = unpack4(r[0]);
#pragma unroll
  for (int i = 1; i < N; ++i) a += unpack4(r[i]);
  const auto s0 = __builtin_amdgcn_permlane32_swap(asu(a[0]), asu(a[2]), false, false), s1 = __builtin_amdgcn_permlane32_swap(asu(a[1]), asu(a[3]), false, false);
  const auto s2 = __builtin_amdgcn_permlane16_swap(asu(asf(s0[0]) + asf(s0[1])), asu(asf(s1[0]) + asf(s1[1])), false, false);
  float t = asf(s2[0]) + asf(s2[1]);
  t += dppf<ROR4>(t); t += dppf<ROR8>(t);
  return t;
}

typedef u32 u32x16 __attribute__((ext_vector_type(16)));
__device__ __forceinline__ const char* mkptr(u32 lo, u32 hi) { return (const char*)(GAS char*)(((uint64_t)hi << 32) | lo); }

__global__ void __launch_bounds__(NTHR, 2) __attribute__((aligned(4096)))
fused_decode(const u16* __restrict__, const u16* __restrict__, unsigned char*, const u16* __restrict__, const u16* __restrict__,
             const u16* __restrict__, const u16* __restrict__, const u16* __restrict__, const u16* __restrict__, u16*) {
  __shared__ __attribute__((aligned(16))) u32 smem[L_END];
  LAS u32* lds = (LAS u32*)smem;
  u32x16 ar; u32x4 ar2;
  asm volatile("s_load_dwordx16 %0, %2, 0x0\n\ts_load_dwordx4 %1, %2, 0x40" : "=&s"(ar), "=&s"(ar2) : "s"(__builtin_amdgcn_kernarg_segment_ptr()));
  SBAR();
  const int lane = threadIdx.x & 63, wave = __builtin_amdgcn_readfirstlane(threadIdx.x >> 6), bid = blockIdx.x;
  {
    uint64_t pc;
    asm volatile("s_cmp_lt_u32 %1, 3\n\ts_cbranch_scc0 1f\n\t"
                 "s_getpc_b64 %0\n\tv_mbcnt_lo_u32_b32 v1, -1, 0\n\tv_mbcnt_hi_u32_b32 v1, -1, v1\n\tv_lshl_add_u32 v1, %1, 6, v1\n\t"
                 "v_lshlrev_b32 v1, 7, v1\n\tv_min_u32 v1, %2, v1\n\tglobal_load_dword v1, v1, %0\n\ts_waitcnt vmcnt(0)\n1:"
                 : "=&s"(pc) : "s"((u32)(wave - 4) | ((u32)((bid >> 4) - (PF0 >> 4)) << 4)), "n"(CODEPF_BYTES) : "scc", "v1");
  }
  const int g = lane >> 4, c = lane & 15, aw = wave - 4;
  const bool isq = bid < 64;
  const u32 stride = isq ? HQ * DH * 2 : FF * 2;
  u32 uo = (288 * (bid & 1) + 72 * aw + 18 * g) * stride + ((bid >> 1) - (isq ? 0 : 32)) * (isq ? 256 : 128) + (c & (isq ? 15 : 7)) * 16;
  asm volatile("" : "+v"(uo));
  SBAR();
  asm volatile("s_waitcnt lgkmcnt(0)" : "+s"(ar), "+s"(ar2));
  const char *kc = mkptr(ar[0], ar[1]), *vc = mkptr(ar[2], ar[3]), *x = mkptr(ar[6], ar[7]), *wq = mkptr(ar[8], ar[9]), *wo = mkptr(ar[10], ar[11]),
             *wg = mkptr(ar[12], ar[13]), *wu = mkptr(ar[14], ar[15]), *wd = mkptr(ar2[0], ar2[1]);
  char *ws = (char*)mkptr(ar[4], ar[5]), *out = (char*)mkptr(ar2[2], ar2[3]);
  const rsrc_t rw = mkrsrc(ws, WS_END);
  const u32x4 rws = {ar[4], ar[5] & 0xffffu, WS_END, 0x00020000u};

  if (__builtin_expect(wave >= 4, 1)) {
    if (__builtin_expect(bid < NU, 1)) {
      const GAS char* gp = (const GAS char*)(c < 8 ? (isq ? wq : wg) : (isq ? wq : wu)) + uo;
      u32x4 a[18];
      u32x3 xr[3];
      unit_load<0, NAUX, 0>(a, gp, stride);
      load_x(mkrsrc(x, 2 * DM * 2), ((lane >> 5) * 288 + (lane & 31) * 3) * 4, xr);
      const u32 qe = __builtin_amdgcn_raw_buffer_load_b32(rw, WS_E, 0, AUX_SC1);
      SBAR();
      unit_load<NAUX, (2 * NAUX < 18 ? 2 * NAUX : 18), NAUX + 4>(a, gp, stride);
      unit_load<2 * NAUX, 18, NAUX>(a, gp, stride);
      STAMP2(24);
      const Sel sel = make_sel(lane);
      LAS u32* xn = lds + L_XN + aw * 576;
      layernorm(xr, xn, lane);
      u32 xm[9];
#pragma unroll
      for (int k = 0; k < 9; ++k) xm[k] = xn[((lane >> 1) & 1) * 288 + 144 * (bid & 1) + 36 * aw + 9 * g + k];
      STAMP2(1);
      f32x4 d = {0.f, 0.f, 0.f, 0.f};
#pragma unroll
      for (int k = 0; k < 18; ++k) d = mfma(a[k], build_b(xm[k >> 1], sel, k & 1), d);
      LAS float* red = (LAS float*)lds + L_RED;
#pragma unroll
      for (int i = 0; i < 4; ++i) red[aw * ACC_W + ((lane >> 1) & 1) * 176 + g * 40 + 8 * i + ((lane >> 2) & 3) * 2 + (lane & 1)] = d[i];
      lds_fence();
      STAMP2(2);
      u32 ticket = 0;
      if (lane == 0) ticket = __hip_atomic_fetch_add(lds + L_CTR, 1u, __ATOMIC_RELAXED, __HIP_MEMORY_SCOPE_WORKGROUP);
      if (__builtin_amdgcn_readfirstlane(ticket) == 3u) {
        asm volatile("" ::: "memory");
        const u32 tag = next_tag(__builtin_amdgcn_readfirstlane(qe));
        const LAS float* p = red + (lane >> 5) * 176 + ((lane >> 3) & 3) * 40 + (lane & 7) * 4;
        const f32x4 s = (*(const LAS f32x4*)p + *(const LAS f32x4*)(p + ACC_W)) + (*(const LAS f32x4*)(p + 2 * ACC_W) + *(const LAS f32x4*)(p + 3 * ACC_W));
        stsc(rw, WS_PG + bid * 1024 + lane * 16, pack4(__builtin_bit_cast(f32x4, __builtin_bit_cast(u32x4, s) + 0x80u), tag));
      }
      STAMP(6);
    }
    if ((bid < O0 || (bid >= Z0 && bid < Z0 + 8)) && aw < 2) {
      const int f = bid < O0 ? bid : bid - (Z0 - O0);
      const u32 oi = (lane >> 5) * DM + 8 * f + 2 * (lane & 3) + (g & 1);
      u32 xv = ((const GAS u16*)x)[oi];
      u32 op = (lane & 12) == 0 ? oi * 2 : OOB;
      u32 go = (aw ? WS_FG : WS_YG) + (f >> 1) * (NF * 128) + (lane >> 2) * 128 + (f & 1) * 64 + (lane & 3) * 16;
      asm volatile("" : "+v"(xv), "+v"(op), "+v"(go));
      wg_barrier();
      STAMPZ(31);
      const u32 tag = lds[L_TAG];
      LAS float* lg = (LAS float*)lds + L_RED + lane;
      const u32 t16 = __builtin_amdgcn_readfirstlane(tag) << 16;
      u32x4 r[5];
      if (aw) {
          gather<5>(r, rws, go, t16);
        *lg = rbf(silu(rbf(sum_parts<5>(r))));
        STAMPZ(7);
        wg_barrier();
        STAMPZ_OUT();
        return;
      }
      gather<4>(r, rws, go, t16);
      STAMP2(3);
      wg_barrier();
      float gt = *lg;
      SBAR();
      float sm = rbf(sum_parts<4>(r));
      asm volatile("" : "+v"(gt), "+v"(sm));
      const u32 res = pk(lo16(xv) + rbf(sm * gt), 0.f);
      __builtin_amdgcn_raw_buffer_store_b16((u16)res, mkrsrc(out, 2 * DM * 2), op, 0, OUT_AUX);
      if (f == 0 && lane == 0) __builtin_amdgcn_raw_buffer_store_b32(tag, rw, WS_E, 0, AUX_SC1);
      STAMPZ(4);
      STAMPZ_OUT();
      return;
    }
    STAMP_OUT();
    return;
  }

  {
    const u32 ko = ((lane >> 2) & 3) * 256 + (4 * g + (lane & 3)) * 16;
    const rsrc_t rk = mkrsrc((const char*)kc + (size_t)bid * CHUNK, CHUNK), rv = mkrsrc((const char*)vc + (size_t)bid * CHUNK, CHUNK);
    const bool odd = lane & 1;
    const u32 vo = lane * 16;
    u32x4 r0[8], r1[8], r2[8], r3[8], qg[2];
    u32 so = wave * 8192, e = 0, qo = 0;
#pragma unroll
    for (int p = 0; p < 32; ++p) {
      if (p == NPRE) { if (bid < NU) { lds[L_CTR] = 0u; wg_barrier(); } else __builtin_amdgcn_s_sleep(IDLE); SBAR(); }
      if (p == NPRO) STAMP2(24);
      if (p == NPRO) {
        u32 ln = lane; asm volatile("" : "+v"(ln));
        qo = WS_PG + ((bid >> 3) & 15) * 4096 + ((ln >> 1) & 1) * 2048 + (bid >> 7) * 512 + (ln >> 2) * 32 + (ln & 1) * 16;
      }
      if (p == QPOS) {
        e = __builtin_amdgcn_raw_buffer_load_b32(rw, WS_E, 0, AUX_SC1);
        qg[0] = ldsc(rw, qo, 0); qg[1] = ldsc(rw, qo, 1024);
      }
      if (p >= QPOS && p < QPOS + NPRO) pace<NPRO + 3>();
      else if (p >= NPRO) pace<NPRO>();
      if (p == NPRO) STAMP2(26);
      if (p == 8 + NPRO) STAMP2(28);
      if (p == 16 + NPRO) STAMP2(29);
      const u32x4 t = ldnt(rk, ko, so + (p >> 3) * 32768 + (p & 7) * 1024);
      if (p < 8) r0[p] = t; else if (p < 16) r1[p - 8] = t; else if (p < 24) r2[p - 16] = t; else r3[p - 24] = t;
      SBAR();
    }
    STAMP(1);
    const u32 tag = next_tag(__builtin_amdgcn_readfirstlane(e));
    if (!tags16_ok<2>(qg, tag)) {
      u32 spins = SPIN_CAP;
      _Pragma("nounroll") do { RELOAD(); qg[0] = ldsc(rw, qo, 0); qg[1] = ldsc(rw, qo, 1024); } while (!tags16_ok<2>(qg, tag) && --spins);
    }
    STAMP(2);
    Sched sc;
    sc.mask = 0xffu; sc.due = now32() + 0x20000000u; sc.tag = tag;
    sc.lead = (bid >= O0 && bid < F0) ? LEAD_O : ((bid >= F0 && bid < Z0) ? LEAD_F : 0);
    sc.ldo = lane < 8 ? WS_SC + lane * SC_STR : OOB;
    sc.sample = bid == 33 * (bid & 7) && wave == 0;
    sc.sto = lane == 0 ? WS_SC + (bid & 7) * SC_STR : OOB;
    LAS f32x4* sl = (LAS f32x4*)(lds + L_S) + wave * 512 + lane;
    float ml;
    {
      const f32x4 qf = unpack4(qg[0]) + unpack4(qg[1]);
      const u32 q0 = pk(qf[0], qf[1]), q1 = pk(qf[2], qf[3]), p0 = dppu<XOR1>(q0), p1 = dppu<XOR1>(q1);
      const u32x4 bq0 = {odd ? 0u : q0, odd ? 0u : q1, odd ? 0u : p0, odd ? 0u : p1}, bq1 = {odd ? p0 : 0u, odd ? p1 : 0u, odd ? q0 : 0u, odd ? q1 : 0u};
      u32x4 u = {0u, 0u, 0u, 0u};
      float mx = -INFINITY;
      for (int it = 0; it < 8; ++it) {
        int itv = it; asm volatile("" : "+s"(itv));
        const bool last = itv == 7;
        const rsrc_t r = mkrsrc((last ? vc : kc) + (size_t)bid * CHUNK, CHUNK);
        const u32 lo = last ? vo : ko;
        so = last ? wave * 8192 : so + 131072;
        f32x4 s4;
        s4[0] = k_tile<0>(r0, bq0, bq1, u, r, lo, so, sc, rw, it);
        s4[1] = k_tile<1>(r1, bq0, bq1, u, r, lo, so, sc, rw, it);
        s4[2] = k_tile<2>(r2, bq0, bq1, u, r, lo, so, sc, rw, it);
        s4[3] = k_tile<3>(r3, bq0, bq1, u, r, lo, so, sc, rw, it);
        sl[it * 64] = s4;
        mx = fmaxf(fmaxf(mx, fmaxf(s4[0], s4[1])), fmaxf(s4[2], s4[3]));
        STAMP(8 + it);
      }
      mx = fmaxf(mx, dppf<XOR1>(mx)); mx = fmaxf(mx, dppf<ROR4>(mx)); mx = fmaxf(mx, dppf<ROR8>(mx));
      ml = max32(max16(mx)) * LOG2E;
    }
    const u32 ho = WS_PG + (64 + 2 * (bid - F0) + ((lane >> 2) & 1)) * 1024 + ((lane >> 1) & 1) * 512 + (lane & 1) * 256 + (4 * wave + g) * 16;
    u32x4 hq = ldsc(rw, (bid >= F0 && bid < Z0) ? ho : OOB, 0);
    float l = 0.f;
    f32x4 acc = {0.f, 0.f, 0.f, 0.f};
    u32x4 bp0 = {0u, 0u, 0u, 0u}, bp1 = {0u, 0u, 0u, 0u};
    const u32 hm = odd ? 0xffff0000u : 0x0000ffffu;
    f32x4 s4 = sl[0];
    for (int it = 8; it < 15; ++it) {
      so += 131072;
      const f32x4 sn = sl[(it - 7) * 64];
      v_tile<0>(r0, probs(s4[0], ml, l, hm), acc, bp0, bp1, rv, vo, so, sc, rw, it);
      v_tile<1>(r1, probs(s4[1], ml, l, hm), acc, bp0, bp1, rv, vo, so, sc, rw, it);
      v_tile<2>(r2, probs(s4[2], ml, l, hm), acc, bp0, bp1, rv, vo, so, sc, rw, it);
      v_tile<3>(r3, probs(s4[3], ml, l, hm), acc, bp0, bp1, rv, vo, so, sc, rw, it);
      s4 = sn;
      STAMP(8 + it);
    }
    const bool isO = bid >= O0 && bid < F0, isF = bid >= F0 && bid < Z0;
    const int u = bid - (isF ? F0 : O0);
    const rsrc_t rl = mkrsrc(isF ? wd : wo, isF ? FF * DM * 2 : (isO ? HQ * DH * DM * 2 : 0));
    const u32 wv = (64 * u + 16 * wave + 4 * g) * 1152 + c * 16, wv4 = c < 8 ? wv : OOB;
    u32x4 a[20];
    v_last<0, NE0, 0>(r0, probs(s4[0], ml, l, hm), acc, bp0, bp1, a, rl, wv, wv4);
    v_last<0, 0, 0>(r1, probs(s4[1], ml, l, hm), acc, bp0, bp1, a, rl, wv, wv4);
    v_last<0, 0, 0>(r2, probs(s4[2], ml, l, hm), acc, bp0, bp1, a, rl, wv, wv4);
    const u32x2 z3 = probs(s4[3], ml, l, hm);
    l += dppf<XOR1>(l); l += dppf<ROR4>(l); l += dppf<ROR8>(l); l = sum32(sum16(l));
    LAS float* lf = (LAS float*)lds;
    if (lane < 4) lf[L_ML + lane * 4 + wave] = odd ? l : ml;
    lds[L_TAG] = tag;
    u32 ad = (L_ACC + wave * ACC_W + ((lane >> 1) & 1) * 176 + g * 40 + ((lane >> 2) & 3) * 2 + (lane & 1)) * 4;
    u32 as = (L_ACC + (lane >> 5) * 176 + ((lane >> 3) & 3) * 40 + (lane & 7) * 4) * 4, am = (L_ML + (lane >> 5) * 8) * 4;
    u32 ao = WS_AG + (((bid >> 3) & 15) * 16 + (lane >> 2)) * 1024 + (((bid >> 1) & 3) * 4 + (bid >> 7) * 2 + (bid & 1)) * 64 + (lane & 3) * 16;
    asm volatile("" : "+v"(ao), "+v"(ad), "+v"(as), "+v"(am));
    SBAR();
    v_last<8 - NE1, NE1, NE0>(r3, z3, acc, bp0, bp1, a, rl, wv, wv4);
    STAMP2(3);
#pragma unroll
    for (int i = 0; i < 4; ++i) *(LAS float*)((LAS char*)lds + ad + 32 * i) = acc[i];
    wg_barrier();
    STAMP(4);
    if (wave == 0) {
      const f32x4 mw = *(const LAS f32x4*)((LAS char*)lds + am), lw = *(const LAS f32x4*)((LAS char*)lds + am + 16);
      f32x4 pa[4];
#pragma unroll
      for (int w = 0; w < 4; ++w) pa[w] = *(const LAS f32x4*)((LAS char*)lds + as + w * (ACC_W * 4));
      SBAR();
      const float M = fmaxf(fmaxf(mw[0], mw[1]), fmaxf(mw[2], mw[3]));
      const f32x4 ew = {ex2(mw[0] - M), ex2(mw[1] - M), ex2(mw[2] - M), ex2(mw[3] - M)};
      const float L = fmaf(ew[0], lw[0], ew[1] * lw[1]) + fmaf(ew[2], lw[2], ew[3] * lw[3]);
      const f32x4 o = (ew[0] * pa[0] + ew[1] * pa[1] + ew[2] * pa[2] + ew[3] * pa[3]) * rcp(L);
      const u32 lse = asu(M + __builtin_amdgcn_logf(L));
      stsc(rw, ao, u32x4{pk(o[0], o[1]), __builtin_amdgcn_perm(tag, lse, 0x05040100u), pk(o[2], o[3]), __builtin_amdgcn_perm(tag, lse, 0x05040302u)});
    }
    STAMP2(5);
    if (isO || isF) {
      rows_load<NE0 + NE1, 20>(a, rl, wv, wv4);
      Sel sel = make_sel(lane);
      u32 po = (isF ? WS_FG : WS_YG) + ((wave * 8 + (lane >> 3)) * NF + u) * 128 + (lane & 7) * 16, po2 = (wave == 0 && lane < 32) ? po + 32 * NF * 128 : OOB;
      u32 yd = L_Y * 4 + (wave * 400 + g * 20 + c) * 16, ys = L_Y * 4 + (wave * 80 + (lane & 3) * 20 + (lane >> 2)) * 16;
      pin_sel(sel);
      asm volatile("" : "+v"(po), "+v"(po2), "+v"(yd), "+v"(ys));
      STAMP(6);
      u32 m0, m1;
      if (isO) {
        const u32 go = WS_AG + (u * 4 + wave) * 1024 + c * 64 + g * 16;
        const u32x4 r = gather1(rws, go, tag << 16);
        STAMP2(7);
        const float lse = asf(__builtin_amdgcn_perm(r[3], r[1], 0x05040100u));
        float M = fmaxf(lse, dppf<XOR1>(lse)); M = fmaxf(M, dppf<ROR4>(M)); M = fmaxf(M, dppf<ROR8>(M));
        const float wt = ex2(lse - M);
        float o[5] = {wt * lo16(r[0]), wt * hi16(r[0]), wt * lo16(r[2]), wt * hi16(r[2]), wt};
#define RED5(C) _Pragma("unroll") for (int i = 0; i < 5; ++i) { o[i] += dppf<C>(o[i]); asm volatile("" : "+v"(o[i])); }
        asm volatile("" : "+v"(o[0]), "+v"(o[1]), "+v"(o[2]), "+v"(o[3]));
        RED5(XOR1) RED5(ROR4) RED5(ROR8)
#undef RED5
        const float inv = rcp(o[4]);
        m0 = pk(o[0] * inv, o[1] * inv); m1 = pk(o[2] * inv, o[3] * inv);
      } else {
        if (!tags16_ok<1>(&hq, tag)) hq = gather1(rws, ho, tag << 16);
        f32x4 h = unpack4(hq);
#pragma unroll
        for (int i = 0; i < 4; ++i) {
          const float own = rbf(h[i] + dppf<ROR4>(h[i])), oth = dppf<XOR1>(own);
          const float gt = odd ? oth : own, up = odd ? own : oth;
          h[i] = rbf(silu(gt)) * up;
        }
        m0 = pk(h[0], h[1]); m1 = pk(h[2], h[3]);
      }
      f32x4 y[5];
      const u32x4 b0 = build_b(m0, sel, 0), b1 = build_b(m0, sel, 1), b2 = build_b(m1, sel, 0), b3 = build_b(m1, sel, 1);
#pragma unroll
      for (int cb = 0; cb < 5; ++cb) y[cb] = mfma(b0, a[cb], f32x4{0.f, 0.f, 0.f, 0.f});
#pragma unroll
      for (int cb = 0; cb < 5; ++cb) y[cb] = mfma(b1, a[5 + cb], y[cb]);
      SBAR();
#pragma unroll
      for (int cb = 0; cb < 5; ++cb) y[cb] = mfma(b2, a[10 + cb], y[cb]);
      SBAR();
#pragma unroll
      for (int cb = 0; cb < 5; ++cb) { y[cb] = mfma(b3, a[15 + cb], y[cb]); SBAR(); }
#pragma unroll
      for (int cb = 0; cb < 5; ++cb) *(LAS f32x4*)((LAS char*)lds + yd + cb * 1280) = y[cb];
      wg_barrier();
      const LAS f32x4* p = (const LAS f32x4*)((LAS char*)lds + ys);
      const f32x4 t0 = p[0], t1 = p[400], t2 = p[800], t3 = p[1200], t4 = p[320], t5 = p[720], t6 = p[1120], t7 = p[1520];
      stsc(rw, po, pack4((t0 + t1) + (t2 + t3), tag));
      stsc(rw, po2, pack4((t4 + t5) + (t6 + t7), tag));
      STAMP(30);
    }
    STAMP_OUT();
    return;
  }
}

extern "C" void kernel_launch(void* const* d_in, const int* in_sizes, int n_in, void* d_out, int out_size, void* d_ws, size_t ws_size,
                              hipStream_t stream) {
  fused_decode<<<NWG, NTHR, 0, stream>>>((const u16*)d_in[1], (const u16*)d_in[2], (unsigned char*)d_ws, (const u16*)d_in[0], (const u16*)d_in[3],
                                          (const u16*)d_in[4], (const u16*)d_in[6], (const u16*)d_in[7], (const u16*)d_in[8], (u16*)d_out);
}
```

```cpp
#include <hip/hip_runtime.h>
#include <hip/hip_bf16.h>
#include <stdint.h>

typedef unsigned u32;
typedef unsigned short u16;
typedef u32 u32x2 __attribute__((ext_vector_type(2)));
typedef u32 u32x3 __attribute__((ext_vector_type(3)));
typedef u32 u32x4 __attribute__((ext_vector_type(4)));
typedef float f32x2 __attribute__((ext_vector_type(2)));
typedef float f32x4 __attribute__((ext_vector_type(4)));
typedef __bf16 bf16x8 __attribute__((ext_vector_type(8)));
typedef __bf16 bf16x2 __attribute__((ext_vector_type(2)));
typedef __amdgpu_buffer_rsrc_t rsrc_t;
#define GAS __attribute__((address_space(1)))
#define LAS __attribute__((address_space(3)))

#define NPRO 5
#define NFL 5
#define NHI 9
#define SCH_DT 219
#define SCH_SH 4
#define SCH_BIAS 24
#define NS 8
#define SC_S 4352
#define SC_MASK 0x4924
#define SCH_KI 0
#define SCH_IMAX 64
#define UPD4 0
#define SCH_TAB 0x0011495557777FFFull
#define SCH_LOW 0x5555555549110100ull
#define LEAD_O 64
#define LEAD_F 32
#define QPOS 26
#define NAUX 9
#define OUT_AUX 16
#define PF0 208
#define NE1 5
#define NPRE 1
#define IDLE 36
#define RELEASE 2
#define TOUCH 7
#define NE0 3
#define NLATE 8
#define CODEPF_BYTES 17408
#define CODEPF 1

constexpr int DM = 576, HQ = 32, SK = 32768, DH = 128, FF = 4608;
constexpr int NWG = 256, NTHR = 512;
constexpr u32 CHUNK = 4096u * DH * 2u;
constexpr int TPW = 32;
constexpr int AUX_NT = 2, AUX_SC1 = 16;
constexpr u32 OOB = 0x80000000u;
constexpr int NU = 208, O0 = 64, F0 = 128, NF = 72, Z0 = 200;
constexpr u32 WS_E = 0, WS_PG = 4096, WS_AG = WS_PG + NU * 1024, WS_YG = WS_AG + NWG * 1024, WS_FG = WS_YG + 36 * NF * 128,
              WS_SC = WS_FG + 36 * NF * 128, SC_STR = SC_S, SC_ROW = SC_S < 128 ? 4352 : NS * SC_S, WS_END = WS_SC + 16 * SC_ROW;
constexpr u32 SPIN_CAP = 400000;
constexpr int L_CTR = 0, L_TAG = 4, L_ML = 8, L_ACC = 32, ACC_W = 352, L_XN = L_ACC + 4 * ACC_W, L_RED = L_XN + 4 * 576, L_Y = L_RED + 4 * ACC_W,
              L_S = L_Y + 4 * 1600, L_END = L_S + 4 * 2048;
constexpr float LOG2E = 1.4426950408889634f;

#define STAMPZ(k)
#define STAMPZ_OUT()
#define STAMP(k)
#define STAMP_OUT()
#define STAMP2(k)

__device__ __forceinline__ rsrc_t mkrsrc(const void* p, u32 bytes) { return __builtin_amdgcn_make_buffer_rsrc((void*)p, 0, bytes, 0x00020000); }
__device__ __forceinline__ u32x4 ldnt(rsrc_t r, u32 vo, u32 so) { return __builtin_amdgcn_raw_buffer_load_b128(r, vo, so, AUX_NT); }
__device__ __forceinline__ u32x4 ldsc(rsrc_t r, u32 vo, u32 so) { return __builtin_amdgcn_raw_buffer_load_b128(r, vo, so, AUX_SC1); }
__device__ __forceinline__ void stsc(rsrc_t r, u32 vo, u32x4 v) { __builtin_amdgcn_raw_buffer_store_b128(v, r, vo, 0, AUX_SC1); }
__device__ __forceinline__ f32x4 mfma(u32x4 a, u32x4 b, f32x4 c) {
  return __builtin_amdgcn_mfma_f32_16x16x32_bf16(__builtin_bit_cast(bf16x8, a), __builtin_bit_cast(bf16x8, b), c, 0, 0, 0);
}
__device__ __forceinline__ float asf(u32 x) { return __builtin_bit_cast(float, x); }
__device__ __forceinline__ u32 asu(float x) { return __builtin_bit_cast(u32, x); }
__device__ __forceinline__ u32 pk(float lo, float hi) { bf16x2 v = {(__bf16)lo, (__bf16)hi}; return __builtin_bit_cast(u32, v); }
__device__ __forceinline__ float rbf(float x) { return (float)(__bf16)x; }
__device__ __forceinline__ float lo16(u32 x) { return asf(x << 16); }
__device__ __forceinline__ float hi16(u32 x) { return asf(x & 0xffff0000u); }
template <int CTRL> __device__ __forceinline__ u32 dppu(u32 x) { return (u32)__builtin_amdgcn_mov_dpp((int)x, CTRL, 0xf, 0xf, true); }
template <int CTRL> __device__ __forceinline__ float dppf(float x) { return asf(dppu<CTRL>(asu(x))); }
constexpr int XOR1 = 0xB1, XOR2 = 0x4E, XOR7 = 0x141, ROR4 = 0x124, ROR8 = 0x128;
__device__ __forceinline__ float sum16(float x) { auto s = __builtin_amdgcn_permlane16_swap(asu(x), asu(x), false, false); return asf(s[0]) + asf(s[1]); }
__device__ __forceinline__ float sum32(float x) { auto s = __builtin_amdgcn_permlane32_swap(asu(x), asu(x), false, false); return asf(s[0]) + asf(s[1]); }
__device__ __forceinline__ float max16(float x) { auto s = __builtin_amdgcn_permlane16_swap(asu(x), asu(x), false, false); return fmaxf(asf(s[0]), asf(s[1])); }
__device__ __forceinline__ float max32(float x) { auto s = __builtin_amdgcn_permlane32_swap(asu(x), asu(x), false, false); return fmaxf(asf(s[0]), asf(s[1])); }
__device__ __forceinline__ float ex2(float x) { return __builtin_amdgcn_exp2f(x); }
__device__ __forceinline__ float rcp(float x) { return __builtin_amdgcn_rcpf(x); }
__device__ __forceinline__ float silu(float x) { return x * rcp(1.0f + ex2(-LOG2E * x)); }
__device__ __forceinline__ u32 now32() { return (u32)__builtin_amdgcn_s_memrealtime(); }
template <int N> __device__ __forceinline__ void pace() { asm volatile("s_waitcnt vmcnt(%0)" ::"n"(N - 1) : "memory"); }
template <int BIT, int EXTRA> __device__ __forceinline__ void pacev(u32 mask) {
  asm volatile("s_bitcmp1_b32 %0, %4+8\n\ts_cbranch_scc0 1f\n\ts_waitcnt vmcnt(%3)\n1:\n\t"
               "s_bitcmp1_b32 %0, %4\n\ts_waitcnt vmcnt(%1)\n\ts_cbranch_scc0 2f\n\ts_waitcnt vmcnt(%2)\n2:"
               ::"s"(mask), "n"(NHI - 1 + EXTRA), "n"(NFL - 1 + EXTRA), "n"(NFL - 2 + EXTRA), "n"(BIT) : "memory", "scc");
}
__device__ __forceinline__ u32 pace_mask(u32 now, u32 due) {
  int up = (int)(now - due) >> SCH_SH, dn = -up;
  asm("s_max_i32 %0, %0, 0\n\ts_min_i32 %0, %0, 7\n\ts_max_i32 %1, %1, 0\n\ts_min_i32 %1, %1, 7" : "+s"(up), "+s"(dn) : : "scc");
  return ((u32)(SCH_TAB >> (8 * up)) & 0xffu) | ((u32)(SCH_LOW >> (8 * dn)) << 8);
}
#define SBAR() __builtin_amdgcn_sched_barrier(0)
#define RELOAD() asm volatile("" ::: "memory")
__device__ __forceinline__ void lds_fence() { asm volatile("s_waitcnt lgkmcnt(0)" ::: "memory"); }
__device__ __forceinline__ void wg_barrier() { lds_fence(); __builtin_amdgcn_s_barrier(); asm volatile("" ::: "memory"); }
__device__ __forceinline__ u32 next_tag(u32 e) { return e * 1664525u + 1013904223u; }

struct Sched { u32 mask, due, on, tag, lead, ldo, sto, prev, cur; int integ; bool sample; u32x2 row; };

template <int W, int T> __device__ __forceinline__ u32 bank_mov(u32 old, u32 z) {
  constexpr int ctrl = W == T ? 0xE4 : (W > T ? 0x110 + 4 * (W - T) : 0x100 + 4 * (T - W));
  return (u32)__builtin_amdgcn_update_dpp((int)old, (int)z, ctrl, 0xf, 1 << W, false);
}
template <int T> __device__ __forceinline__ void build_p(u32x4& b, u32 z) {
  b[0] = bank_mov<0, T>(b[0], z); b[1] = bank_mov<1, T>(b[1], z); b[2] = bank_mov<2, T>(b[2], z); b[3] = bank_mov<3, T>(b[3], z);
}

template <int GRP> __device__ __forceinline__ void sched_step(Sched& sc, u32 now, rsrc_t rw, int it) {
  if (GRP == 0) {
    sc.row = __builtin_amdgcn_raw_buffer_load_b64(rw, ((SC_MASK >> it) & 1) ? sc.ldo : OOB, it * SC_ROW, AUX_SC1);
    if (sc.sample && ((SC_MASK >> (it + 1)) & 1)) __builtin_amdgcn_raw_buffer_store_b64(u32x2{now + sc.lead, sc.tag}, rw, sc.sto, (it + 1) * SC_ROW, AUX_SC1);
    sc.prev = sc.cur; sc.cur = now;
  } else if (GRP == (UPD4 ? 1 : 2)) {
    const bool ok = __builtin_amdgcn_ballot_w64(sc.row[1] == sc.tag) == (1ull << NS) - 1;
    u32 d = sc.row[0] - now;
    d += dppu<XOR1>(d); d += dppu<XOR2>(d); d += dppu<XOR7>(d);
    if (NS == 16) d += dppu<ROR8>(d);
    const u32 mean = now + ((int)__builtin_amdgcn_readfirstlane(d) >> (NS == 16 ? 4 : 3)) - sc.lead;
    if (ok) {
      if (SCH_KI && sc.on) {
        sc.integ += (int)(sc.prev - mean) >> SCH_KI;
        asm("s_max_i32 %0, %0, %1\n\ts_min_i32 %0, %0, %2" : "+s"(sc.integ) : "n"(-SCH_IMAX), "n"(SCH_IMAX) : "scc");
      }
      sc.due = mean + ((UPD4 ? 5 * SCH_DT / 2 : 3 * SCH_DT) - SCH_BIAS) - sc.integ; sc.on = 1;
    }
  }
  sc.mask = sc.on ? pace_mask(now, sc.due) : 0xffu;
  sc.due += UPD4 ? (SCH_DT + (GRP & 1)) / 2 : SCH_DT;
}
template <int GRP, int I>
__device__ __forceinline__ void refill(u32x4& slot, rsrc_t r, u32 vo, u32 so, Sched& sc, u32& now, rsrc_t rw, int it) {
  pacev<I, (GRP == 0 && I >= 2 && I < 2 + NFL) ? 1 : 0>(sc.mask);
  slot = ldnt(r, vo, so + GRP * 32768 + I * 1024);
  if ((UPD4 || !(GRP & 1)) && I == 0) now = now32();
  if ((UPD4 || !(GRP & 1)) && I == 1) sched_step<GRP>(sc, now, rw, it);
  SBAR();
}
template <int T> __device__ __forceinline__ void k_pick(u32x4& u, f32x4 d) {
  u[0] = bank_mov<T, 0>(u[0], asu(d[0])); u[1] = bank_mov<T, 1>(u[1], asu(d[1])); u[2] = bank_mov<T, 2>(u[2], asu(d[2])); u[3] = bank_mov<T, 3>(u[3], asu(d[3]));
}
template <int GRP>
__device__ __forceinline__ float k_tile(u32x4 (&k)[8], u32x4 bq0, u32x4 bq1, u32x4& u, rsrc_t r, u32 vo, u32 so, Sched& sc, rsrc_t rw, int it) {
  const f32x4 z = {0.f, 0.f, 0.f, 0.f};
  f32x4 da, db;
  u32 now = 0;
#define RF(I) SBAR(); refill<GRP, I>(k[I], r, vo, so, sc, now, rw, it);
  da = mfma(k[0], bq0, z);                     RF(0)
  da = mfma(k[1], bq1, da);                    RF(1)
  db = mfma(k[2], bq0, z);                     RF(2)
  db = mfma(k[3], bq1, db); k_pick<0>(u, da);  RF(3)
  da = mfma(k[4], bq0, z);                     RF(4)
  da = mfma(k[5], bq1, da); k_pick<1>(u, db);  RF(5)
  db = mfma(k[6], bq0, z);                     RF(6)
  db = mfma(k[7], bq1, db); k_pick<2>(u, da);  RF(7)
#undef RF
  k_pick<3>(u, db);
  const float s = (asf(u[0]) + asf(u[1])) + (asf(u[2]) + asf(u[3]));
  return rbf(rbf(s) * 0.08838834764831845f);
}
__device__ __forceinline__ u32x2 probs(float s, float ml, float& l, u32 hm) {
  const float p = ex2(fmaf(s, LOG2E, -ml));
  l += p;
  const u32 t = pk(p, p);
  return u32x2{dppu<0xA0>(t) & hm, dppu<0xF5>(t) & hm};
}
template <int GRP>
__device__ __forceinline__ void v_tile(u32x4 (&v)[8], u32x2 z, f32x4& acc, u32x4& bp0, u32x4& bp1, rsrc_t r, u32 vo, u32 so, Sched& sc, rsrc_t rw, int it) {
  u32 now = 0;
#define PV_STEP(U, BP) build_p<(U) / 2>(BP, z[(U) & 1]); acc = mfma(v[U], BP, acc); SBAR(); refill<GRP, U>(v[U], r, vo, so, sc, now, rw, it);
  PV_STEP(0, bp0) PV_STEP(1, bp1) PV_STEP(2, bp0) PV_STEP(3, bp1) PV_STEP(4, bp0) PV_STEP(5, bp1) PV_STEP(6, bp0) PV_STEP(7, bp1)
#undef PV_STEP
}
template <int K> __device__ __forceinline__ u32x4 row_load(rsrc_t r, u32 vo, u32 vo4) { return ldnt(r, K % 5 == 4 ? vo4 : vo, (K / 5) * 1152 + (K % 5) * 256); }
template <int FROM, int CNT, int K0>
__device__ __forceinline__ void v_last(const u32x4 (&v)[8], u32x2 z, f32x4& acc, u32x4& bp0, u32x4& bp1, u32x4 (&a)[20], rsrc_t r, u32 vo, u32 vo4) {
#define PV_STEP(U, BP) build_p<(U) / 2>(BP, z[(U) & 1]); acc = mfma(v[U], BP, acc); SBAR(); \
  if constexpr (U >= FROM && U < FROM + CNT) { a[K0 + U - FROM] = row_load<K0 + U - FROM>(r, vo, vo4); SBAR(); }
  PV_STEP(0, bp0) PV_STEP(1, bp1) PV_STEP(2, bp0) PV_STEP(3, bp1) PV_STEP(4, bp0) PV_STEP(5, bp1) PV_STEP(6, bp0) PV_STEP(7, bp1)
#undef PV_STEP
}
template <int LO, int HI> __device__ __forceinline__ void rows_load(u32x4 (&a)[20], rsrc_t r, u32 vo, u32 vo4) {
  if constexpr (LO < HI) { pace<NLATE>(); a[LO] = row_load<LO>(r, vo, vo4); SBAR(); rows_load<LO + 1, HI>(a, r, vo, vo4); }
}

struct Sel { u32 s[4][2]; };
__device__ __forceinline__ Sel make_sel(int lane) {
  Sel r; const int wp = (lane >> 2) & 3; const bool hb = lane & 1;
#pragma unroll
  for (int par = 0; par < 2; ++par) {
    const u32 two = ((2 * par + 1) << 8) | (2 * par);
    const u32 base = hb ? ((two << 16) | 0x0c0cu) : (0x0c0c0000u | two);
#pragma unroll
    for (int w = 0; w < 4; ++w) r.s[w][par] = wp == w ? base : 0x0c0c0c0cu;
  }
  return r;
}
__device__ __forceinline__ void pin_sel(Sel& s) {
  asm volatile("" : "+v"(s.s[0][0]), "+v"(s.s[0][1]), "+v"(s.s[1][0]), "+v"(s.s[1][1]), "+v"(s.s[2][0]), "+v"(s.s[2][1]), "+v"(s.s[3][0]), "+v"(s.s[3][1]));
}
__device__ __forceinline__ u32x4 build_b(u32 d, const Sel& sel, int par) {
  u32x4 b;
#pragma unroll
  for (int w = 0; w < 4; ++w) b[w] = __builtin_amdgcn_perm(d, d, sel.s[w][par]);
  return b;
}
__device__ __forceinline__ void load_x(rsrc_t rx, u32 xo, u32x3 (&xr)[3]) {
#pragma unroll
  for (int k = 0; k < 3; ++k) xr[k] = __builtin_amdgcn_raw_buffer_load_b96(rx, xo, k * 384, 0);
}
template <int LO, int HI, int N> __device__ __forceinline__ void unit_load(u32x4 (&a)[18], const GAS char*& gp, u32 stride) {
  if constexpr (LO < HI) {
    if constexpr (N > 0) pace<N>();
    a[LO] = __builtin_nontemporal_load((const GAS u32x4*)gp);
    gp += stride;
    if constexpr (LO == 17 - RELEASE) __builtin_amdgcn_s_barrier();
    SBAR();
    unit_load<LO + 1, HI, N>(a, gp, stride);
  }
}
__device__ __forceinline__ void layernorm(const u32x3 (&xr)[3], LAS u32* xn, int lane) {
  f32x2 v[9], s2 = {0.f, 0.f};
#pragma unroll
  for (int k = 0; k < 9; ++k) { v[k] = f32x2{lo16(xr[k / 3][k % 3]), hi16(xr[k / 3][k % 3])}; s2 += v[k]; }
  float s = s2[0] + s2[1];
  s += dppf<XOR1>(s); s += dppf<XOR2>(s); s += dppf<ROR4>(s); s += dppf<ROR8>(s); s = sum16(s);
  const float mean = s * (1.0f / DM);
  f32x2 q2 = {0.f, 0.f};
#pragma unroll
  for (int k = 0; k < 9; ++k) { const f32x2 a = v[k] - mean; q2 += a * a; }
  float q = q2[0] + q2[1];
  q += dppf<XOR1>(q); q += dppf<XOR2>(q); q += dppf<ROR4>(q); q += dppf<ROR8>(q); q = sum16(q);
  const float mb = rbf(mean), den = rbf(__builtin_amdgcn_sqrtf(rbf(rbf(q * (1.0f / DM)) + 1e-5f))), inv = rcp(den);
#pragma unroll
  for (int k = 0; k < 9; ++k) {
    const f32x2 a = v[k] - mb;
    const u32 r = pk(a[0], a[1]);
    const f32x2 b = f32x2{lo16(r), hi16(r)} * inv;
    xn[(lane >> 5) * 288 + (lane & 31) * 3 + 96 * (k / 3) + k % 3] = pk(b[0], b[1]);
  }
  lds_fence();
}
__device__ __forceinline__ u32x4 pack4(f32x4 v, u32 tag) {
  return u32x4{__builtin_amdgcn_perm(asu(v[1]), asu(v[0]), 0x05030201u), __builtin_amdgcn_perm(tag, asu(v[1]), 0x05040302u),
               __builtin_amdgcn_perm(asu(v[3]), asu(v[2]), 0x05030201u), __builtin_amdgcn_perm(tag, asu(v[3]), 0x05040302u)};
}
__device__ __forceinline__ f32x4 unpack4(u32x4 r) {
  return f32x4{asf(r[0] << 8), asf(__builtin_amdgcn_perm(r[1], r[0], 0x0504030cu)), asf(r[2] << 8), asf(__builtin_amdgcn_perm(r[3], r[2], 0x0504030cu))};
}
template <int N> __device__ __forceinline__ bool tags16_ok(const u32x4* r, u32 tag) {
  u32 bad = 0;
#pragma unroll
  for (int i = 0; i < N; ++i) bad |= (r[i][1] ^ (tag << 16)) | (r[i][3] ^ (tag << 16));
  return __builtin_amdgcn_ballot_w64(bad > 0xffffu) == 0ull;
}
typedef unsigned long long u64;
#define G_LD(M, R, SO, OFF) "s_wqm_b64 exec, " M "\n\tbuffer_load_dwordx4 " R ", %[vo], %[rs], " SO " offen " OFF " sc1\n\t"
#define G_CK(M, A, B) "v_bitop3_b32 %[t], " A ", " B ", %[tg] bitop3:0x7e\n\tv_cmp_lt_u32_e64 " M ", %[lim], %[t]\n\t"
#define G_END "s_cbranch_scc0 2f\n\ts_sub_u32 %[n], %[n], 1\n\ts_cbranch_scc0 1b\n2:"
__device__ __forceinline__ u32x4 gather1(u32x4 rs, u32 vo, u32 t16) {
  u32x4 r; u64 m0; u32 t, n = SPIN_CAP;
  asm volatile("1:\n\tbuffer_load_dwordx4 v[140:143], %[vo], %[rs], 0 offen sc1\n\ts_waitcnt vmcnt(0)\n\t" G_CK("%[m0]", "v141", "v143")
               "s_cmp_lg_u64 %[m0], 0\n\t" G_END
               : "=&{v[140:143]}"(r), [m0] "=&s"(m0), [t] "=&v"(t), [n] "+s"(n) : [vo] "v"(vo), [rs] "s"(rs), [tg] "s"(t16), [lim] "v"(0xffffu) : "memory", "scc");
  return r;
}
template <int N> __device__ __forceinline__ void gather(u32x4 (&r)[5], u32x4 rs, u32 vo, u32 t16) {
  u64 m0 = ~0ull, m1 = ~0ull, m2 = ~0ull, m3 = ~0ull, m4 = N > 4 ? 0xffffffffull : 0ull, any; u32 t, n = SPIN_CAP;
  r[4] = u32x4{0u, t16, 0u, t16};
  asm volatile("1:\n\t" G_LD("%[m0]", "v[40:43]", "0", "") G_LD("%[m1]", "v[44:47]", "0", "offset:2048") G_LD("%[m2]", "v[48:51]", "%[s4]", "")
               G_LD("%[m3]", "v[52:55]", "%[s4]", "offset:2048") ".if %[five]\n\t" G_LD("%[m4]", "v[56:59]", "%[s8]", "") ".endif\n\t"
               "s_mov_b64 exec, -1\n\ts_waitcnt vmcnt(0)\n\t"
               G_CK("%[m0]", "v41", "v43") G_CK("%[m1]", "v45", "v47") G_CK("%[m2]", "v49", "v51") G_CK("%[m3]", "v53", "v55")
               ".if %[five]\n\t" G_CK("%[m4]", "v57", "v59") ".endif\n\t"
               "s_or_b64 %[any], %[m0], %[m1]\n\ts_or_b64 %[any], %[any], %[m2]\n\ts_or_b64 %[any], %[any], %[m3]\n\t"
               ".if %[five]\n\ts_or_b64 %[any], %[any], %[m4]\n\t.endif\n\t" G_END
               : "=&{v[40:43]}"(r[0]), "=&{v[44:47]}"(r[1]), "=&{v[48:51]}"(r[2]), "=&{v[52:55]}"(r[3]), "+{v[56:59]}"(r[4]), [m0] "+s"(m0), [m1] "+s"(m1),
                 [m2] "+s"(m2), [m3] "+s"(m3), [m4] "+s"(m4), [any] "=&s"(any), [t] "=&v"(t), [n] "+s"(n)
               : [vo] "v"(vo), [rs] "s"(rs), [tg] "s"(t16), [lim] "v"(0xffffu), [s4] "s"(4096u), [s8] "s"(8192u), [five] "n"(N > 4) : "memory", "scc");
}
template <int N> __device__ __forceinline__ float sum_parts(const u32x4* r) {
  f32x4 a = unpack4(r[0]);
#pragma unroll
  for (int i = 1; i < N; ++i) a += unpack4(r[i]);
  const auto s0 = __builtin_amdgcn_permlane32_swap(asu(a[0]), asu(a[2]), false, false), s1 = __builtin_amdgcn_permlane32_swap(asu(a[1]), asu(a[3]), false, false);
  const auto s2 = __builtin_amdgcn_permlane16_swap(asu(asf(s0[0]) + asf(s0[1])), asu(asf(s1[0]) + asf(s1[1])), false, false);
  float t = asf(s2[0]) + asf(s2[1]);
  t += dppf<ROR4>(t); t += dppf<ROR8>(t);
  return t;
}

typedef u32 u32x16 __attribute__((ext_vector_type(16)));
__device__ __forceinline__ const char* mkptr(u32 lo, u32 hi) { return (const char*)(GAS char*)(((uint64_t)hi << 32) | lo); }

__global__ void __launch_bounds__(NTHR, 2) __attribute__((aligned(4096)))
fused_decode(const u16* __restrict__, const u16* __restrict__, unsigned char*, const u16* __restrict__, const u16* __restrict__,
             const u16* __restrict__, const u16* __restrict__, const u16* __restrict__, const u16* __restrict__, u16*) {
  __shared__ __attribute__((aligned(16))) u32 smem[L_END];
  LAS u32* lds = (LAS u32*)smem;
  u32x16 ar; u32x4 ar2;
  asm volatile("s_load_dwordx16 %0, %2, 0x0\n\ts_load_dwordx4 %1, %2, 0x40" : "=&s"(ar), "=&s"(ar2) : "s"(__builtin_amdgcn_kernarg_segment_ptr()));
  SBAR();
  const int lane = threadIdx.x & 63, wave = __builtin_amdgcn_readfirstlane(threadIdx.x >> 6), bid = blockIdx.x;
  {
    uint64_t pc;
    asm volatile("s_cmp_lt_u32 %1, 3\n\ts_cbranch_scc0 1f\n\t"
                 "s_getpc_b64 %0\n\tv_mbcnt_lo_u32_b32 v1, -1, 0\n\tv_mbcnt_hi_u32_b32 v1, -1, v1\n\tv_lshl_add_u32 v1, %1, 6, v1\n\t"
                 "v_lshlrev_b32 v1, 7, v1\n\tv_min_u32 v1, %2, v1\n\tglobal_load_dword v1, v1, %0\n\ts_waitcnt vmcnt(0)\n1:"
                 : "=&s"(pc) : "s"((u32)(wave - 4) | ((u32)((bid >> 4) - (PF0 >> 4)) << 4)), "n"(CODEPF_BYTES) : "scc", "v1");
  }
  const int g = lane >> 4, c = lane & 15, aw = wave - 4;
  const bool isq = bid < 64;
  const u32 stride = isq ? HQ * DH * 2 : FF * 2;
  u32 uo = (288 * (bid & 1) + 72 * aw + 18 * g) * stride + ((bid >> 1) - (isq ? 0 : 32)) * (isq ? 256 : 128) + (c & (isq ? 15 : 7)) * 16;
  asm volatile("" : "+v"(uo));
  SBAR();
  asm volatile("s_waitcnt lgkmcnt(0)" : "+s"(ar), "+s"(ar2));
  const char *kc = mkptr(ar[0], ar[1]), *vc = mkptr(ar[2], ar[3]), *x = mkptr(ar[6], ar[7]), *wq = mkptr(ar[8], ar[9]), *wo = mkptr(ar[10], ar[11]),
             *wg = mkptr(ar[12], ar[13]), *wu = mkptr(ar[14], ar[15]), *wd = mkptr(ar2[0], ar2[1]);
  char *ws = (char*)mkptr(ar[4], ar[5]), *out = (char*)mkptr(ar2[2], ar2[3]);
  const rsrc_t rw = mkrsrc(ws, WS_END);
  const u32x4 rws = {ar[4], ar[5] & 0xffffu, WS_END, 0x00020000u};

  if (__builtin_expect(wave >= 4, 1)) {
    if (__builtin_expect(bid < NU, 1)) {
      const GAS char* gp = (const GAS char*)(c < 8 ? (isq ? wq : wg) : (isq ? wq : wu)) + uo;
      u32x4 a[18];
      u32x3 xr[3];
      unit_load<0, NAUX, 0>(a, gp, stride);
      load_x(mkrsrc(x, 2 * DM * 2), ((lane >> 5) * 288 + (lane & 31) * 3) * 4, xr);
      const u32 qe = __builtin_amdgcn_raw_buffer_load_b32(rw, WS_E, 0, AUX_SC1);
      SBAR();
      unit_load<NAUX, (2 * NAUX < 18 ? 2 * NAUX : 18), NAUX + 4>(a, gp, stride);
      unit_load<2 * NAUX, 18, NAUX>(a, gp, stride);
      STAMP2(24);
      const Sel sel = make_sel(lane);
      LAS u32* xn = lds + L_XN + aw * 576;
      layernorm(xr, xn, lane);
      u32 xm[9];
#pragma unroll
      for (int k = 0; k < 9; ++k) xm[k] = xn[((lane >> 1) & 1) * 288 + 144 * (bid & 1) + 36 * aw + 9 * g + k];
      STAMP2(1);
      f32x4 d = {0.f, 0.f, 0.f, 0.f};
#pragma unroll
      for (int k = 0; k < 18; ++k) d = mfma(a[k], build_b(xm[k >> 1], sel, k & 1), d);
      LAS float* red = (LAS float*)lds + L_RED;
#pragma unroll
      for (int i = 0; i < 4; ++i) red[aw * ACC_W + ((lane >> 1) & 1) * 176 + g * 40 + 8 * i + ((lane >> 2) & 3) * 2 + (lane & 1)] = d[i];
      lds_fence();
      STAMP2(2);
      u32 ticket = 0;
      if (lane == 0) ticket = __hip_atomic_fetch_add(lds + L_CTR, 1u, __ATOMIC_RELAXED, __HIP_MEMORY_SCOPE_WORKGROUP);
      if (__builtin_amdgcn_readfirstlane(ticket) == 3u) {
        asm volatile("" ::: "memory");
        const u32 tag = next_tag(__builtin_amdgcn_readfirstlane(qe));
        const LAS float* p = red + (lane >> 5) * 176 + ((lane >> 3) & 3) * 40 + (lane & 7) * 4;
        const f32x4 s = (*(const LAS f32x4*)p + *(const LAS f32x4*)(p + ACC_W)) + (*(const LAS f32x4*)(p + 2 * ACC_W) + *(const LAS f32x4*)(p + 3 * ACC_W));
        stsc(rw, WS_PG + bid * 1024 + lane * 16, pack4(__builtin_bit_cast(f32x4, __builtin_bit_cast(u32x4, s) + 0x80u), tag));
      }
      STAMP(6);
    }
    if ((bid < O0 || (bid >= Z0 && bid < Z0 + 8)) && aw < 2) {
      const int f = bid < O0 ? bid : bid - (Z0 - O0);
      const u32 oi = (lane >> 5) * DM + 8 * f + 2 * (lane & 3) + (g & 1);
      u32 xv = ((const GAS u16*)x)[oi];
      u32 op = (lane & 12) == 0 ? oi * 2 : OOB;
      u32 go = (aw ? WS_FG : WS_YG) + (f >> 1) * (NF * 128) + (lane >> 2) * 128 + (f & 1) * 64 + (lane & 3) * 16;
      if (TOUCH & 4) { const u32 t = __builtin_amdgcn_raw_buffer_load_b16(mkrsrc(out, 2 * DM * 2), op, 0, AUX_SC1); asm volatile("" ::"v"(t)); }
      asm volatile("" : "+v"(xv), "+v"(op), "+v"(go));
      wg_barrier();
      STAMPZ(31);
      const u32 tag = lds[L_TAG];
      LAS float* lg = (LAS float*)lds + L_RED + lane;
      const u32 t16 = __builtin_amdgcn_readfirstlane(tag) << 16;
      u32x4 r[5];
      if (aw) {
          gather<5>(r, rws, go, t16);
        *lg = rbf(silu(rbf(sum_parts<5>(r))));
        STAMPZ(7);
        wg_barrier();
        STAMPZ_OUT();
        return;
      }
      gather<4>(r, rws, go, t16);
      STAMP2(3);
      wg_barrier();
      float gt = *lg;
      SBAR();
      float sm = rbf(sum_parts<4>(r));
      asm volatile("" : "+v"(gt), "+v"(sm));
      const u32 res = pk(lo16(xv) + rbf(sm * gt), 0.f);
      __builtin_amdgcn_raw_buffer_store_b16((u16)res, mkrsrc(out, 2 * DM * 2), op, 0, OUT_AUX);
      if (f == 0 && lane == 0) __builtin_amdgcn_raw_buffer_store_b32(tag, rw, WS_E, 0, AUX_SC1);
      STAMPZ(4);
      STAMPZ_OUT();
      return;
    }
    STAMP_OUT();
    return;
  }

  {
    const u32 ko = ((lane >> 2) & 3) * 256 + (4 * g + (lane & 3)) * 16;
    const rsrc_t rk = mkrsrc((const char*)kc + (size_t)bid * CHUNK, CHUNK), rv = mkrsrc((const char*)vc + (size_t)bid * CHUNK, CHUNK);
    const bool odd = lane & 1;
    const u32 vo = lane * 16;
    u32x4 r0[8], r1[8], r2[8], r3[8], qg[2];
    u32 so = wave * 8192, e = 0, qo = 0;
#pragma unroll
    for (int p = 0; p < 32; ++p) {
      if (p == NPRE) { if (bid < NU) { lds[L_CTR] = 0u; wg_barrier(); } else __builtin_amdgcn_s_sleep(IDLE); SBAR(); }
      if (p == NPRO) STAMP2(24);
      if (p == NPRO) {
        u32 ln = lane; asm volatile("" : "+v"(ln));
        qo = WS_PG + ((bid >> 3) & 15) * 4096 + ((ln >> 1) & 1) * 2048 + (bid >> 7) * 512 + (ln >> 2) * 32 + (ln & 1) * 16;
      }
      if (p == QPOS) {
        e = __builtin_amdgcn_raw_buffer_load_b32(rw, WS_E, 0, AUX_SC1);
        qg[0] = ldsc(rw, qo, 0); qg[1] = ldsc(rw, qo, 1024);
      }
      if (p >= QPOS && p < QPOS + NPRO) pace<NPRO + 3>();
      else if (p >= NPRO) pace<NPRO>();
      if (p == NPRO) STAMP2(26);
      if (p == 8 + NPRO) STAMP2(28);
      if (p == 16 + NPRO) STAMP2(29);
      const u32x4 t = ldnt(rk, ko, so + (p >> 3) * 32768 + (p & 7) * 1024);
      if (p < 8) r0[p] = t; else if (p < 16) r1[p - 8] = t; else if (p < 24) r2[p - 16] = t; else r3[p - 24] = t;
      SBAR();
    }
    STAMP(1);
    const u32 tag = next_tag(__builtin_amdgcn_readfirstlane(e));
    if (!tags16_ok<2>(qg, tag)) {
      u32 spins = SPIN_CAP;
      _Pragma("nounroll") do { RELOAD(); qg[0] = ldsc(rw, qo, 0); qg[1] = ldsc(rw, qo, 1024); } while (!tags16_ok<2>(qg, tag) && --spins);
    }
    STAMP(2);
    Sched sc;
    sc.mask = 0xffu; sc.due = 0u; sc.on = 0u; sc.tag = tag; sc.prev = sc.cur = 0u; sc.integ = 0;
    sc.lead = (bid >= O0 && bid < F0) ? LEAD_O : ((bid >= F0 && bid < Z0) ? LEAD_F : 0);
    sc.ldo = lane < NS ? WS_SC + lane * SC_STR : OOB;
    sc.sample = bid == (NS == 16 ? 17 : 33) * (bid & (NS - 1)) && wave == (bid & 3);
    sc.sto = lane == 0 ? WS_SC + (bid & (NS - 1)) * SC_STR : OOB;
    u32 tv = 0, tw = 0;
    if (TOUCH & 1) tv = __builtin_amdgcn_raw_buffer_load_b32(rv, (wave == 0 && lane < 2) ? lane * (CHUNK - 4) : OOB, 0, AUX_NT);
    LAS f32x4* sl = (LAS f32x4*)(lds + L_S) + wave * 512 + lane;
    float ml;
    {
      const f32x4 qf = unpack4(qg[0]) + unpack4(qg[1]);
      const u32 q0 = pk(qf[0], qf[1]), q1 = pk(qf[2], qf[3]), p0 = dppu<XOR1>(q0), p1 = dppu<XOR1>(q1);
      const u32x4 bq0 = {odd ? 0u : q0, odd ? 0u : q1, odd ? 0u : p0, odd ? 0u : p1}, bq1 = {odd ? p0 : 0u, odd ? p1 : 0u, odd ? q0 : 0u, odd ? q1 : 0u};
      u32x4 u = {0u, 0u, 0u, 0u};
      float mx = -INFINITY;
      for (int it = 0; it < 8; ++it) {
        int itv = it; asm volatile("" : "+s"(itv));
        const bool last = itv == 7;
        const rsrc_t r = mkrsrc((last ? vc : kc) + (size_t)bid * CHUNK, CHUNK);
        const u32 lo = last ? vo : ko;
        so = last ? wave * 8192 : so + 131072;
        f32x4 s4;
        s4[0] = k_tile<0>(r0, bq0, bq1, u, r, lo, so, sc, rw, it);
        s4[1] = k_tile<1>(r1, bq0, bq1, u, r, lo, so, sc, rw, it);
        s4[2] = k_tile<2>(r2, bq0, bq1, u, r, lo, so, sc, rw, it);
        s4[3] = k_tile<3>(r3, bq0, bq1, u, r, lo, so, sc, rw, it);
        sl[it * 64] = s4;
        mx = fmaxf(fmaxf(mx, fmaxf(s4[0], s4[1])), fmaxf(s4[2], s4[3]));
        STAMP(8 + it);
      }
      mx = fmaxf(mx, dppf<XOR1>(mx)); mx = fmaxf(mx, dppf<ROR4>(mx)); mx = fmaxf(mx, dppf<ROR8>(mx));
      ml = max32(max16(mx)) * LOG2E;
    }
    const u32 ho = WS_PG + (64 + 2 * (bid - F0) + ((lane >> 2) & 1)) * 1024 + ((lane >> 1) & 1) * 512 + (lane & 1) * 256 + (4 * wave + g) * 16;
    u32x4 hq = ldsc(rw, (bid >= F0 && bid < Z0) ? ho : OOB, 0);
    const bool isO = bid >= O0 && bid < F0, isF = bid >= F0 && bid < Z0;
    const int u = bid - (isF ? F0 : O0);
    const rsrc_t rl = mkrsrc(isF ? wd : wo, isF ? FF * DM * 2 : (isO ? HQ * DH * DM * 2 : 0));
    if (TOUCH & 1) asm volatile("" ::"v"(tv));
    if (TOUCH & 2) tw = __builtin_amdgcn_raw_buffer_load_b32(rl, (wave == 0 && lane < 2) ? (64 * u + 64 * lane) * 1152 - 4 * lane : OOB, 0, AUX_NT);
    float l = 0.f;
    f32x4 acc = {0.f, 0.f, 0.f, 0.f};
    u32x4 bp0 = {0u, 0u, 0u, 0u}, bp1 = {0u, 0u, 0u, 0u};
    const u32 hm = odd ? 0xffff0000u : 0x0000ffffu;
    f32x4 s4 = sl[0];
    for (int it = 8; it < 15; ++it) {
      so += 131072;
      const f32x4 sn = sl[(it - 7) * 64];
      v_tile<0>(r0, probs(s4[0], ml, l, hm), acc, bp0, bp1, rv, vo, so, sc, rw, it);
      v_tile<1>(r1, probs(s4[1], ml, l, hm), acc, bp0, bp1, rv, vo, so, sc, rw, it);
      v_tile<2>(r2, probs(s4[2], ml, l, hm), acc, bp0, bp1, rv, vo, so, sc, rw, it);
      v_tile<3>(r3, probs(s4[3], ml, l, hm), acc, bp0, bp1, rv, vo, so, sc, rw, it);
      s4 = sn;
      STAMP(8 + it);
    }
    if (TOUCH & 2) asm volatile("" ::"v"(tw));
    const u32 wv = (64 * u + 16 * wave + 4 * g) * 1152 + c * 16, wv4 = c < 8 ? wv : OOB;
    u32x4 a[20];
    v_last<0, NE0, 0>(r0, probs(s4[0], ml, l, hm), acc, bp0, bp1, a, rl, wv, wv4);
    v_last<0, 0, 0>(r1, probs(s4[1], ml, l, hm), acc, bp0, bp1, a, rl, wv, wv4);
    v_last<0, 0, 0>(r2, probs(s4[2], ml, l, hm), acc, bp0, bp1, a, rl, wv, wv4);
    const u32x2 z3 = probs(s4[3], ml, l, hm);
    l += dppf<XOR1>(l); l += dppf<ROR4>(l); l += dppf<ROR8>(l); l = sum32(sum16(l));
    LAS float* lf = (LAS float*)lds;
    if (lane < 4) lf[L_ML + lane * 4 + wave] = odd ? l : ml;
    lds[L_TAG] = tag;
    u32 ad = (L_ACC + wave * ACC_W + ((lane >> 1) & 1) * 176 + g * 40 + ((lane >> 2) & 3) * 2 + (lane & 1)) * 4;
    u32 as = (L_ACC + (lane >> 5) * 176 + ((lane >> 3) & 3) * 40 + (lane & 7) * 4) * 4, am = (L_ML + (lane >> 5) * 8) * 4;
    u32 ao = WS_AG + (((bid >> 3) & 15) * 16 + (lane >> 2)) * 1024 + (((bid >> 1) & 3) * 4 + (bid >> 7) * 2 + (bid & 1)) * 64 + (lane & 3) * 16;
    asm volatile("" : "+v"(ao), "+v"(ad), "+v"(as), "+v"(am));
    SBAR();
    v_last<8 - NE1, NE1, NE0>(r3, z3, acc, bp0, bp1, a, rl, wv, wv4);
    STAMP2(3);
#pragma unroll
    for (int i = 0; i < 4; ++i) *(LAS float*)((LAS char*)lds + ad + 32 * i) = acc[i];
    wg_barrier();
    STAMP(4);
    if (wave == 0) {
      const f32x4 mw = *(const LAS f32x4*)((LAS char*)lds + am), lw = *(const LAS f32x4*)((LAS char*)lds + am + 16);
      f32x4 pa[4];
#pragma unroll
      for (int w = 0; w < 4; ++w) pa[w] = *(const LAS f32x4*)((LAS char*)lds + as + w * (ACC_W * 4));
      SBAR();
      const float M = fmaxf(fmaxf(mw[0], mw[1]), fmaxf(mw[2], mw[3]));
      const f32x4 ew = {ex2(mw[0] - M), ex2(mw[1] - M), ex2(mw[2] - M), ex2(mw[3] - M)};
      const float L = fmaf(ew[0], lw[0], ew[1] * lw[1]) + fmaf(ew[2], lw[2], ew[3] * lw[3]);
      const f32x4 o = (ew[0] * pa[0] + ew[1] * pa[1] + ew[2] * pa[2] + ew[3] * pa[3]) * rcp(L);
      const u32 lse = asu(M + __builtin_amdgcn_logf(L));
      stsc(rw, ao, u32x4{pk(o[0], o[1]), __builtin_amdgcn_perm(tag, lse, 0x05040100u), pk(o[2], o[3]), __builtin_amdgcn_perm(tag, lse, 0x05040302u)});
    }
    STAMP2(5);
    if (isO || isF) {
      rows_load<NE0 + NE1, 20>(a, rl, wv, wv4);
      Sel sel = make_sel(lane);
      u32 po = (isF ? WS_FG : WS_YG) + ((wave * 8 + (lane >> 3)) * NF + u) * 128 + (lane & 7) * 16, po2 = (wave == 0 && lane < 32) ? po + 32 * NF * 128 : OOB;
      u32 yd = L_Y * 4 + (wave * 400 + g * 20 + c) * 16, ys = L_Y * 4 + (wave * 80 + (lane & 3) * 20 + (lane >> 2)) * 16;
      pin_sel(sel);
      asm volatile("" : "+v"(po), "+v"(po2), "+v"(yd), "+v"(ys));
      STAMP(6);
      u32 m0, m1;
      if (isO) {
        const u32 go = WS_AG + (u * 4 + wave) * 1024 + c * 64 + g * 16;
        const u32x4 r = gather1(rws, go, tag << 16);
        STAMP2(7);
        const float lse = asf(__builtin_amdgcn_perm(r[3], r[1], 0x05040100u));
        float M = fmaxf(lse, dppf<XOR1>(lse)); M = fmaxf(M, dppf<ROR4>(M)); M = fmaxf(M, dppf<ROR8>(M));
        const float wt = ex2(lse - M);
        float o[5] = {wt * lo16(r[0]), wt * hi16(r[0]), wt * lo16(r[2]), wt * hi16(r[2]), wt};
#define RED5(C) _Pragma("unroll") for (int i = 0; i < 5; ++i) { o[i] += dppf<C>(o[i]); asm volatile("" : "+v"(o[i])); }
        asm volatile("" : "+v"(o[0]), "+v"(o[1]), "+v"(o[2]), "+v"(o[3]));
        RED5(XOR1) RED5(ROR4) RED5(ROR8)
#undef RED5
        const float inv = rcp(o[4]);
        m0 = pk(o[0] * inv, o[1] * inv); m1 = pk(o[2] * inv, o[3] * inv);
      } else {
        if (!tags16_ok<1>(&hq, tag)) hq = gather1(rws, ho, tag << 16);
        f32x4 h = unpack4(hq);
#pragma unroll
        for (int i = 0; i < 4; ++i) {
          const float own = rbf(h[i] + dppf<ROR4>(h[i])), oth = dppf<XOR1>(own);
          const float gt = odd ? oth : own, up = odd ? own : oth;
          h[i] = rbf(silu(gt)) * up;
        }
        m0 = pk(h[0], h[1]); m1 = pk(h[2], h[3]);
      }
      f32x4 y[5];
      const u32x4 b0 = build_b(m0, sel, 0), b1 = build_b(m0, sel, 1), b2 = build_b(m1, sel, 0), b3 = build_b(m1, sel, 1);
#pragma unroll
      for (int cb = 0; cb < 5; ++cb) y[cb] = mfma(b0, a[cb], f32x4{0.f, 0.f, 0.f, 0.f});
#pragma unroll
      for (int cb = 0; cb < 5; ++cb) y[cb] = mfma(b1, a[5 + cb], y[cb]);
      SBAR();
#pragma unroll
      for (int cb = 0; cb < 5; ++cb) y[cb] = mfma(b2, a[10 + cb], y[cb]);
      SBAR();
#pragma unroll
      for (int cb = 0; cb < 5; ++cb) { y[cb] = mfma(b3, a[15 + cb], y[cb]); SBAR(); }
#pragma unroll
      for (int cb = 0; cb < 5; ++cb) *(LAS f32x4*)((LAS char*)lds + yd + cb * 1280) = y[cb];
      wg_barrier();
      const LAS f32x4* p = (const LAS f32x4*)((LAS char*)lds + ys);
      const f32x4 t0 = p[0], t1 = p[400], t2 = p[800], t3 = p[1200], t4 = p[320], t5 = p[720], t6 = p[1120], t7 = p[1520];
      stsc(rw, po, pack4((t0 + t1) + (t2 + t3), tag));
      stsc(rw, po2, pack4((t4 + t5) + (t6 + t7), tag));
      STAMP(30);
    }
    STAMP_OUT();
    return;
  }
}

extern "C" void kernel_launch(void* const* d_in, const int* in_sizes, int n_in, void* d_out, int out_size, void* d_ws, size_t ws_size,
                              hipStream_t stream) {
  fused_decode<<<NWG, NTHR, 0, stream>>>((const u16*)d_in[1], (const u16*)d_in[2], (unsigned char*)d_ws, (const u16*)d_in[0], (const u16*)d_in[3],
                                          (const u16*)d_in[4], (const u16*)d_in[6], (const u16*)d_in[7], (const u16*)d_in[8], (u16*)d_out);
}
```

```cpp
#include <hip/hip_runtime.h>
#include <hip/hip_bf16.h>
#include <stdint.h>

typedef unsigned u32;
typedef unsigned short u16;
typedef u32 u32x2 __attribute__((ext_vector_type(2)));
typedef u32 u32x3 __attribute__((ext_vector_type(3)));
typedef u32 u32x4 __attribute__((ext_vector_type(4)));
typedef float f32x2 __attribute__((ext_vector_type(2)));
typedef float f32x4 __attribute__((ext_vector_type(4)));
typedef __bf16 bf16x8 __attribute__((ext_vector_type(8)));
typedef __bf16 bf16x2 __attribute__((ext_vector_type(2)));
typedef __amdgpu_buffer_rsrc_t rsrc_t;
#define GAS __attribute__((address_space(1)))
#define LAS __attribute__((address_space(3)))

#define NPRO 5
#define NFL 5
#define NHI 9
#define SCH_DT 212
#define SCH_SH 4
#define SCH_BIAS 20
#define NS 8
#define SC_S 4352
#define SC_MASK 0x4924
#define SCH_TAB 0x0011495557777FFFull
#define SCH_LOW 0x5555555549110100ull
#define LEAD_O 96
#define LEAD_F 80
#define QPOS 26
#define NAUX 9
#define PF0 208
#define NE1 5
#define NPRE 1
#define IDLE 36
#define RELEASE 2
#define MAPA 0x2024, 0x1a4, 0x1c8, 0x6004, 0x4400, 0x34ac, 0x2a0c, 0x2024, 0x2120, 0x190, 0x180, 0x100, 0x3284, 0x3130, 0x4080, 0x408, 0x1d5c, 0x84c
#define MAPC 0x22, 0x8, 0x14, 0x1, 0x10, 0x2, 0x40, 0x80
#define SPG 3
#define SPI 2
#define ADT 2
#define NE0 3
#define NLATE 8
#define CODEPF_BYTES 18048
#define CODEPF 1

constexpr int DM = 576, HQ = 32, SK = 32768, DH = 128, FF = 4608;
constexpr int NWG = 256, NTHR = 512;
constexpr u32 CHUNK = 4096u * DH * 2u;
constexpr u32 PAIR = 8 * CHUNK;
constexpr u32 MA[18] = {MAPA}, MC[8] = {MAPC};
template <int LO, int N> __host__ __device__ constexpr u32 mapa(u32 v) { u32 r = 0; for (int j = 0; j < N; ++j) r ^= ((v >> j) & 1) ? MA[LO + j] << 8 : 0u; return r; }
constexpr int TPW = 32;
constexpr int AUX_NT = 2, AUX_SC1 = 16;
constexpr u32 OOB = 0x80000000u;
constexpr int NU = 208, O0 = 64, F0 = 128, NF = 72, Z0 = 200;
constexpr u32 WS_E = 0, WS_PG = 4096, WS_AG = WS_PG + NU * 1024, WS_YG = WS_AG + NWG * 1024, WS_FG = WS_YG + 36 * NF * 128,
              WS_SC = WS_FG + 36 * NF * 128, SC_STR = SC_S, SC_ROW = SC_S < 128 ? 4352 : NS * SC_S, WS_END = WS_SC + 16 * SC_ROW;
constexpr u32 SPIN_CAP = 400000;
constexpr int L_CTR = 0, L_TAG = 4, L_ML = 8, L_ACC = 32, ACC_W = 352, L_XN = L_ACC + 4 * ACC_W, L_RED = L_XN + 4 * 576, L_Y = L_RED + 4 * ACC_W,
              L_S = L_Y + 4 * 1600, L_END = L_S + 4 * 2048;
constexpr float LOG2E = 1.4426950408889634f;

#define STAMPZ(k)
#define STAMPZ_OUT()
#define STAMP(k)
#define STAMP_OUT()
#define STAMP2(k)

__device__ __forceinline__ rsrc_t mkrsrc(const void* p, u32 bytes) { return __builtin_amdgcn_make_buffer_rsrc((void*)p, 0, bytes, 0x00020000); }
__device__ __forceinline__ u32x4 ldnt(rsrc_t r, u32 vo, u32 so) { return __builtin_amdgcn_raw_buffer_load_b128(r, vo, so, AUX_NT); }
__device__ __forceinline__ u32x4 ldsc(rsrc_t r, u32 vo, u32 so) { return __builtin_amdgcn_raw_buffer_load_b128(r, vo, so, AUX_SC1); }
__device__ __forceinline__ void stsc(rsrc_t r, u32 vo, u32x4 v) { __builtin_amdgcn_raw_buffer_store_b128(v, r, vo, 0, AUX_SC1); }
__device__ __forceinline__ f32x4 mfma(u32x4 a, u32x4 b, f32x4 c) {
  return __builtin_amdgcn_mfma_f32_16x16x32_bf16(__builtin_bit_cast(bf16x8, a), __builtin_bit_cast(bf16x8, b), c, 0, 0, 0);
}
__device__ __forceinline__ float asf(u32 x) { return __builtin_bit_cast(float, x); }
__device__ __forceinline__ u32 asu(float x) { return __builtin_bit_cast(u32, x); }
__device__ __forceinline__ u32 pk(float lo, float hi) { bf16x2 v = {(__bf16)lo, (__bf16)hi}; return __builtin_bit_cast(u32, v); }
__device__ __forceinline__ float rbf(float x) { return (float)(__bf16)x; }
__device__ __forceinline__ float lo16(u32 x) { return asf(x << 16); }
__device__ __forceinline__ float hi16(u32 x) { return asf(x & 0xffff0000u); }
template <int CTRL> __device__ __forceinline__ u32 dppu(u32 x) { return (u32)__builtin_amdgcn_mov_dpp((int)x, CTRL, 0xf, 0xf, true); }
template <int CTRL> __device__ __forceinline__ float dppf(float x) { return asf(dppu<CTRL>(asu(x))); }
constexpr int XOR1 = 0xB1, XOR2 = 0x4E, XOR7 = 0x141, ROR4 = 0x124, ROR8 = 0x128;
__device__ __forceinline__ float sum16(float x) { auto s = __builtin_amdgcn_permlane16_swap(asu(x), asu(x), false, false); return asf(s[0]) + asf(s[1]); }
__device__ __forceinline__ float sum32(float x) { auto s = __builtin_amdgcn_permlane32_swap(asu(x), asu(x), false, false); return asf(s[0]) + asf(s[1]); }
__device__ __forceinline__ float max16(float x) { auto s = __builtin_amdgcn_permlane16_swap(asu(x), asu(x), false, false); return fmaxf(asf(s[0]), asf(s[1])); }
__device__ __forceinline__ float max32(float x) { auto s = __builtin_amdgcn_permlane32_swap(asu(x), asu(x), false, false); return fmaxf(asf(s[0]), asf(s[1])); }
__device__ __forceinline__ float ex2(float x) { return __builtin_amdgcn_exp2f(x); }
__device__ __forceinline__ float rcp(float x) { return __builtin_amdgcn_rcpf(x); }
__device__ __forceinline__ float silu(float x) { return x * rcp(1.0f + ex2(-LOG2E * x)); }
__device__ __forceinline__ u32 now32() { return (u32)__builtin_amdgcn_s_memrealtime(); }
template <int N> __device__ __forceinline__ void pace() { asm volatile("s_waitcnt vmcnt(%0)" ::"n"(N - 1) : "memory"); }
template <int BIT, int EXTRA> __device__ __forceinline__ void pacev(u32 mask) {
  asm volatile("s_bitcmp1_b32 %0, %4+8\n\ts_cbranch_scc0 1f\n\ts_waitcnt vmcnt(%3)\n1:\n\t"
               "s_bitcmp1_b32 %0, %4\n\ts_waitcnt vmcnt(%1)\n\ts_cbranch_scc0 2f\n\ts_waitcnt vmcnt(%2)\n2:"
               ::"s"(mask), "n"(NHI - 1 + EXTRA), "n"(NFL - 1 + EXTRA), "n"(NFL - 2 + EXTRA), "n"(BIT) : "memory", "scc");
}
__device__ __forceinline__ u32 pace_mask(u32 now, u32 due) {
  int up = (int)(now - due) >> SCH_SH, dn = -up;
  asm("s_max_i32 %0, %0, 0\n\ts_min_i32 %0, %0, 7\n\ts_max_i32 %1, %1, 0\n\ts_min_i32 %1, %1, 7" : "+s"(up), "+s"(dn) : : "scc");
  return ((u32)(SCH_TAB >> (8 * up)) & 0xffu) | ((u32)(SCH_LOW >> (8 * dn)) << 8);
}
#define SBAR() __builtin_amdgcn_sched_barrier(0)
#define RELOAD() asm volatile("" ::: "memory")
__device__ __forceinline__ void lds_fence() { asm volatile("s_waitcnt lgkmcnt(0)" ::: "memory"); }
__device__ __forceinline__ void wg_barrier() { lds_fence(); __builtin_amdgcn_s_barrier(); asm volatile("" ::: "memory"); }
__device__ __forceinline__ u32 next_tag(u32 e) { return e * 1664525u + 1013904223u; }

struct Sched { u32 mask, due, on, tag, lead, ldo, sto, prev, dt; bool sample; u32x2 row; };

template <int W, int T> __device__ __forceinline__ u32 bank_mov(u32 old, u32 z) {
  constexpr int ctrl = W == T ? 0xE4 : (W > T ? 0x110 + 4 * (W - T) : 0x100 + 4 * (T - W));
  return (u32)__builtin_amdgcn_update_dpp((int)old, (int)z, ctrl, 0xf, 1 << W, false);
}
template <int T> __device__ __forceinline__ void build_p(u32x4& b, u32 z) {
  b[0] = bank_mov<0, T>(b[0], z); b[1] = bank_mov<1, T>(b[1], z); b[2] = bank_mov<2, T>(b[2], z); b[3] = bank_mov<3, T>(b[3], z);
}

template <int GRP> __device__ __forceinline__ void sched_step(Sched& sc, u32 now, rsrc_t rw, int it) {
  if (GRP == 0) {
    sc.row = __builtin_amdgcn_raw_buffer_load_b64(rw, ((SC_MASK >> it) & 1) ? sc.ldo : OOB, it * SC_ROW, AUX_SC1);
    if (sc.sample && ((SC_MASK >> (it + 1)) & 1)) __builtin_amdgcn_raw_buffer_store_b64(u32x2{now + sc.lead, sc.tag}, rw, sc.sto, (it + 1) * SC_ROW, AUX_SC1);
  } else {
    const bool ok = __builtin_amdgcn_ballot_w64(sc.row[1] == sc.tag) == (1ull << NS) - 1;
    u32 d = sc.row[0] - now;
    d += dppu<XOR1>(d); d += dppu<XOR2>(d); d += dppu<XOR7>(d);
    if (NS == 16) d += dppu<ROR8>(d);
    const u32 mean = now + ((int)__builtin_amdgcn_readfirstlane(d) >> (NS == 16 ? 4 : 3)) - sc.lead;
    if (ok) {
      if (ADT && sc.on == (u32)it - 3u) {
        int m = (int)((mean - sc.prev) * 683u) >> 12;
        if (ADT == 2) m = (m + (int)sc.dt) >> 1;
        asm("s_max_i32 %0, %0, %1\n\ts_min_i32 %0, %0, %2" : "+s"(m) : "n"(SCH_DT - 8), "n"(SCH_DT + 24) : "scc");
        sc.dt = m;
      }
      sc.prev = mean; sc.on = it;
      sc.due = mean + 3 * sc.dt - SCH_BIAS;
    }
  }
  sc.mask = sc.on ? pace_mask(now, sc.due) : 0xffu;
  sc.due += sc.dt;
}
template <int GRP, int I>
__device__ __forceinline__ void refill(u32x4& slot, rsrc_t r, u32 vo, u32 so, Sched& sc, u32& now, rsrc_t rw, int it) {
  pacev<I, (GRP == 0 && I >= 2 && I < 2 + NFL) ? 1 : 0>(sc.mask);
  constexpr u32 step = mapa<10, 5>(GRP * 8 + I);
  slot = ldnt(r, vo, so ^ step);
  if (!(GRP & 1) && I == 0) now = now32();
  if (!(GRP & 1) && I == 1) sched_step<GRP>(sc, now, rw, it);
  if (SPG && GRP == SPG && I == SPI && it == 14 && sc.lead == 0) sc.mask = 0u;
  SBAR();
}
template <int T> __device__ __forceinline__ void k_pick(u32x4& u, f32x4 d) {
  u[0] = bank_mov<T, 0>(u[0], asu(d[0])); u[1] = bank_mov<T, 1>(u[1], asu(d[1])); u[2] = bank_mov<T, 2>(u[2], asu(d[2])); u[3] = bank_mov<T, 3>(u[3], asu(d[3]));
}
template <int GRP>
__device__ __forceinline__ float k_tile(u32x4 (&k)[8], u32x4 bq0, u32x4 bq1, u32x4& u, rsrc_t r, u32 vo, u32 so, Sched& sc, rsrc_t rw, int it) {
  const f32x4 z = {0.f, 0.f, 0.f, 0.f};
  f32x4 da, db;
  u32 now = 0;
#define RF(I) SBAR(); refill<GRP, I>(k[I], r, vo, so, sc, now, rw, it);
  da = mfma(k[0], bq0, z);                     RF(0)
  da = mfma(k[1], bq1, da);                    RF(1)
  db = mfma(k[2], bq0, z);                     RF(2)
  db = mfma(k[3], bq1, db); k_pick<0>(u, da);  RF(3)
  da = mfma(k[4], bq0, z);                     RF(4)
  da = mfma(k[5], bq1, da); k_pick<1>(u, db);  RF(5)
  db = mfma(k[6], bq0, z);                     RF(6)
  db = mfma(k[7], bq1, db); k_pick<2>(u, da);  RF(7)
#undef RF
  k_pick<3>(u, db);
  const float s = (asf(u[0]) + asf(u[1])) + (asf(u[2]) + asf(u[3]));
  return rbf(rbf(s) * 0.08838834764831845f);
}
__device__ __forceinline__ u32x2 probs(float s, float ml, float& l, u32 hm) {
  const float p = ex2(fmaf(s, LOG2E, -ml));
  l += p;
  const u32 t = pk(p, p);
  return u32x2{dppu<0xA0>(t) & hm, dppu<0xF5>(t) & hm};
}
template <int GRP>
__device__ __forceinline__ void v_tile(u32x4 (&v)[8], u32x2 z, f32x4& acc, u32x4& bp0, u32x4& bp1, rsrc_t r, u32 vo, u32 so, Sched& sc, rsrc_t rw, int it) {
  u32 now = 0;
#define PV_STEP(U, BP) build_p<(U) / 2>(BP, z[(U) & 1]); acc = mfma(v[U], BP, acc); SBAR(); refill<GRP, U>(v[U], r, vo, so, sc, now, rw, it);
  PV_STEP(0, bp0) PV_STEP(1, bp1) PV_STEP(2, bp0) PV_STEP(3, bp1) PV_STEP(4, bp0) PV_STEP(5, bp1) PV_STEP(6, bp0) PV_STEP(7, bp1)
#undef PV_STEP
}
template <int K> __device__ __forceinline__ u32x4 row_load(rsrc_t r, u32 vo, u32 vo4) { return ldnt(r, K % 5 == 4 ? vo4 : vo, (K / 5) * 1152 + (K % 5) * 256); }
template <int FROM, int CNT, int K0>
__device__ __forceinline__ void v_last(const u32x4 (&v)[8], u32x2 z, f32x4& acc, u32x4& bp0, u32x4& bp1, u32x4 (&a)[20], rsrc_t r, u32 vo, u32 vo4) {
#define PV_STEP(U, BP) build_p<(U) / 2>(BP, z[(U) & 1]); acc = mfma(v[U], BP, acc); SBAR(); \
  if constexpr (U >= FROM && U < FROM + CNT) { a[K0 + U - FROM] = row_load<K0 + U - FROM>(r, vo, vo4); SBAR(); }
  PV_STEP(0, bp0) PV_STEP(1, bp1) PV_STEP(2, bp0) PV_STEP(3, bp1) PV_STEP(4, bp0) PV_STEP(5, bp1) PV_STEP(6, bp0) PV_STEP(7, bp1)
#undef PV_STEP
}
template <int LO, int HI> __device__ __forceinline__ void rows_load(u32x4 (&a)[20], rsrc_t r, u32 vo, u32 vo4) {
  if constexpr (LO < HI) { pace<NLATE>(); a[LO] = row_load<LO>(r, vo, vo4); SBAR(); rows_load<LO + 1, HI>(a, r, vo, vo4); }
}

struct Sel { u32 s[4][2]; };
__device__ __forceinline__ Sel make_sel(int lane) {
  Sel r; const int wp = (lane >> 2) & 3; const bool hb = lane & 1;
#pragma unroll
  for (int par = 0; par < 2; ++par) {
    const u32 two = ((2 * par + 1) << 8) | (2 * par);
    const u32 base = hb ? ((two << 16) | 0x0c0cu) : (0x0c0c0000u | two);
#pragma unroll
    for (int w = 0; w < 4; ++w) r.s[w][par] = wp == w ? base : 0x0c0c0c0cu;
  }
  return r;
}
__device__ __forceinline__ void pin_sel(Sel& s) {
  asm volatile("" : "+v"(s.s[0][0]), "+v"(s.s[0][1]), "+v"(s.s[1][0]), "+v"(s.s[1][1]), "+v"(s.s[2][0]), "+v"(s.s[2][1]), "+v"(s.s[3][0]), "+v"(s.s[3][1]));
}
__device__ __forceinline__ u32x4 build_b(u32 d, const Sel& sel, int par) {
  u32x4 b;
#pragma unroll
  for (int w = 0; w < 4; ++w) b[w] = __builtin_amdgcn_perm(d, d, sel.s[w][par]);
  return b;
}
__device__ __forceinline__ void load_x(rsrc_t rx, u32 xo, u32x3 (&xr)[3]) {
#pragma unroll
  for (int k = 0; k < 3; ++k) xr[k] = __builtin_amdgcn_raw_buffer_load_b96(rx, xo, k * 384, 0);
}
template <int LO, int HI, int N> __device__ __forceinline__ void unit_load(u32x4 (&a)[18], const GAS char*& gp, u32 stride) {
  if constexpr (LO < HI) {
    if constexpr (N > 0) pace<N>();
    a[LO] = __builtin_nontemporal_load((const GAS u32x4*)gp);
    gp += stride;
    if constexpr (LO == 17 - RELEASE) __builtin_amdgcn_s_barrier();
    SBAR();
    unit_load<LO + 1, HI, N>(a, gp, stride);
  }
}
__device__ __forceinline__ void layernorm(const u32x3 (&xr)[3], LAS u32* xn, int lane) {
  f32x2 v[9], s2 = {0.f, 0.f};
#pragma unroll
  for (int k = 0; k < 9; ++k) { v[k] = f32x2{lo16(xr[k / 3][k % 3]), hi16(xr[k / 3][k % 3])}; s2 += v[k]; }
  float s = s2[0] + s2[1];
  s += dppf<XOR1>(s); s += dppf<XOR2>(s); s += dppf<ROR4>(s); s += dppf<ROR8>(s); s = sum16(s);
  const float mean = s * (1.0f / DM);
  f32x2 q2 = {0.f, 0.f};
#pragma unroll
  for (int k = 0; k < 9; ++k) { const f32x2 a = v[k] - mean; q2 += a * a; }
  float q = q2[0] + q2[1];
  q += dppf<XOR1>(q); q += dppf<XOR2>(q); q += dppf<ROR4>(q); q += dppf<ROR8>(q); q = sum16(q);
  const float mb = rbf(mean), den = rbf(__builtin_amdgcn_sqrtf(rbf(rbf(q * (1.0f / DM)) + 1e-5f))), inv = rcp(den);
#pragma unroll
  for (int k = 0; k < 9; ++k) {
    const f32x2 a = v[k] - mb;
    const u32 r = pk(a[0], a[1]);
    const f32x2 b = f32x2{lo16(r), hi16(r)} * inv;
    xn[(lane >> 5) * 288 + (lane & 31) * 3 + 96 * (k / 3) + k % 3] = pk(b[0], b[1]);
  }
  lds_fence();
}
__device__ __forceinline__ u32x4 pack4(f32x4 v, u32 tag) {
  return u32x4{__builtin_amdgcn_perm(asu(v[1]), asu(v[0]), 0x05030201u), __builtin_amdgcn_perm(tag, asu(v[1]), 0x05040302u),
               __builtin_amdgcn_perm(asu(v[3]), asu(v[2]), 0x05030201u), __builtin_amdgcn_perm(tag, asu(v[3]), 0x05040302u)};
}
__device__ __forceinline__ f32x4 unpack4(u32x4 r) {
  return f32x4{asf(r[0] << 8), asf(__builtin_amdgcn_perm(r[1], r[0], 0x0504030cu)), asf(r[2] << 8), asf(__builtin_amdgcn_perm(r[3], r[2], 0x0504030cu))};
}
template <int N> __device__ __forceinline__ bool tags16_ok(const u32x4* r, u32 tag) {
  u32 bad = 0;
#pragma unroll
  for (int i = 0; i < N; ++i) bad |= (r[i][1] ^ (tag << 16)) | (r[i][3] ^ (tag << 16));
  return __builtin_amdgcn_ballot_w64(bad > 0xffffu) == 0ull;
}
typedef unsigned long long u64;
#define G_LD(M, R, SO, OFF) "s_wqm_b64 exec, " M "\n\tbuffer_load_dwordx4 " R ", %[vo], %[rs], " SO " offen " OFF " sc1\n\t"
#define G_CK(M, A, B) "v_bitop3_b32 %[t], " A ", " B ", %[tg] bitop3:0x7e\n\tv_cmp_lt_u32_e64 " M ", %[lim], %[t]\n\t"
#define G_END "s_cbranch_scc0 2f\n\ts_sub_u32 %[n], %[n], 1\n\ts_cbranch_scc0 1b\n2:"
__device__ __forceinline__ u32x4 gather1(u32x4 rs, u32 vo, u32 t16) {
  u32x4 r; u64 m0; u32 t, n = SPIN_CAP;
  asm volatile("1:\n\tbuffer_load_dwordx4 v[140:143], %[vo], %[rs], 0 offen sc1\n\ts_waitcnt vmcnt(0)\n\t" G_CK("%[m0]", "v141", "v143")
               "s_cmp_lg_u64 %[m0], 0\n\t" G_END
               : "=&{v[140:143]}"(r), [m0] "=&s"(m0), [t] "=&v"(t), [n] "+s"(n) : [vo] "v"(vo), [rs] "s"(rs), [tg] "s"(t16), [lim] "v"(0xffffu) : "memory", "scc");
  return r;
}
template <int N> __device__ __forceinline__ void gather(u32x4 (&r)[5], u32x4 rs, u32 vo, u32 t16) {
  u64 m0 = ~0ull, m1 = ~0ull, m2 = ~0ull, m3 = ~0ull, m4 = N > 4 ? 0xffffffffull : 0ull, any; u32 t, n = SPIN_CAP;
  r[4] = u32x4{0u, t16, 0u, t16};
  asm volatile("1:\n\t" G_LD("%[m0]", "v[40:43]", "0", "") G_LD("%[m1]", "v[44:47]", "0", "offset:2048") G_LD("%[m2]", "v[48:51]", "%[s4]", "")
               G_LD("%[m3]", "v[52:55]", "%[s4]", "offset:2048") ".if %[five]\n\t" G_LD("%[m4]", "v[56:59]", "%[s8]", "") ".endif\n\t"
               "s_mov_b64 exec, -1\n\ts_waitcnt vmcnt(0)\n\t"
               G_CK("%[m0]", "v41", "v43") G_CK("%[m1]", "v45", "v47") G_CK("%[m2]", "v49", "v51") G_CK("%[m3]", "v53", "v55")
               ".if %[five]\n\t" G_CK("%[m4]", "v57", "v59") ".endif\n\t"
               "s_or_b64 %[any], %[m0], %[m1]\n\ts_or_b64 %[any], %[any], %[m2]\n\ts_or_b64 %[any], %[any], %[m3]\n\t"
               ".if %[five]\n\ts_or_b64 %[any], %[any], %[m4]\n\t.endif\n\t" G_END
               : "=&{v[40:43]}"(r[0]), "=&{v[44:47]}"(r[1]), "=&{v[48:51]}"(r[2]), "=&{v[52:55]}"(r[3]), "+{v[56:59]}"(r[4]), [m0] "+s"(m0), [m1] "+s"(m1),
                 [m2] "+s"(m2), [m3] "+s"(m3), [m4] "+s"(m4), [any] "=&s"(any), [t] "=&v"(t), [n] "+s"(n)
               : [vo] "v"(vo), [rs] "s"(rs), [tg] "s"(t16), [lim] "v"(0xffffu), [s4] "s"(4096u), [s8] "s"(8192u), [five] "n"(N > 4) : "memory", "scc");
}
template <int N> __device__ __forceinline__ float sum_parts(const u32x4* r) {
  f32x4 a = unpack4(r[0]);
#pragma unroll
  for (int i = 1; i < N; ++i) a += unpack4(r[i]);
  const auto s0 = __builtin_amdgcn_permlane32_swap(asu(a[0]), asu(a[2]), false, false), s1 = __builtin_amdgcn_permlane32_swap(asu(a[1]), asu(a[3]), false, false);
  const auto s2 = __builtin_amdgcn_permlane16_swap(asu(asf(s0[0]) + asf(s0[1])), asu(asf(s1[0]) + asf(s1[1])), false, false);
  float t = asf(s2[0]) + asf(s2[1]);
  t += dppf<ROR4>(t); t += dppf<ROR8>(t);
  return t;
}

typedef u32 u32x16 __attribute__((ext_vector_type(16)));
__device__ __forceinline__ const char* mkptr(u32 lo, u32 hi) { return (const char*)(GAS char*)(((uint64_t)hi << 32) | lo); }

__global__ void __launch_bounds__(NTHR, 2) __attribute__((aligned(4096)))
fused_decode(const u16* __restrict__, const u16* __restrict__, unsigned char*, const u16* __restrict__, const u16* __restrict__,
             const u16* __restrict__, const u16* __restrict__, const u16* __restrict__, const u16* __restrict__, u16*) {
  __shared__ __attribute__((aligned(16))) u32 smem[L_END];
  LAS u32* lds = (LAS u32*)smem;
  u32x16 ar; u32x4 ar2;
  asm volatile("s_load_dwordx16 %0, %2, 0x0\n\ts_load_dwordx4 %1, %2, 0x40" : "=&s"(ar), "=&s"(ar2) : "s"(__builtin_amdgcn_kernarg_segment_ptr()));
  SBAR();
  const int lane = threadIdx.x & 63, wave = __builtin_amdgcn_readfirstlane(threadIdx.x >> 6), bid = blockIdx.x;
  {
    uint64_t pc;
    asm volatile("s_cmp_lt_u32 %1, 3\n\ts_cbranch_scc0 1f\n\t"
                 "s_getpc_b64 %0\n\tv_mbcnt_lo_u32_b32 v1, -1, 0\n\tv_mbcnt_hi_u32_b32 v1, -1, v1\n\tv_lshl_add_u32 v1, %1, 6, v1\n\t"
                 "v_lshlrev_b32 v1, 7, v1\n\tv_min_u32 v1, %2, v1\n\tglobal_load_dword v1, v1, %0\n\ts_waitcnt vmcnt(0)\n1:"
                 : "=&s"(pc) : "s"((u32)(wave - 4) | ((u32)((bid >> 4) - (PF0 >> 4)) << 4)), "n"(CODEPF_BYTES) : "scc", "v1");
  }
  const int g = lane >> 4, c = lane & 15, aw = wave - 4;
  const bool isq = bid < 64;
  const u32 stride = isq ? HQ * DH * 2 : FF * 2;
  u32 uo = (288 * (bid & 1) + 72 * aw + 18 * g) * stride + ((bid >> 1) - (isq ? 0 : 32)) * (isq ? 256 : 128) + (c & (isq ? 15 : 7)) * 16;
  asm volatile("" : "+v"(uo));
  SBAR();
  asm volatile("s_waitcnt lgkmcnt(0)" : "+s"(ar), "+s"(ar2));
  const char *kc = mkptr(ar[0], ar[1]), *vc = mkptr(ar[2], ar[3]), *x = mkptr(ar[6], ar[7]), *wq = mkptr(ar[8], ar[9]), *wo = mkptr(ar[10], ar[11]),
             *wg = mkptr(ar[12], ar[13]), *wu = mkptr(ar[14], ar[15]), *wd = mkptr(ar2[0], ar2[1]);
  char *ws = (char*)mkptr(ar[4], ar[5]), *out = (char*)mkptr(ar2[2], ar2[3]);
  const rsrc_t rw = mkrsrc(ws, WS_END);
  const u32x4 rws = {ar[4], ar[5] & 0xffffu, WS_END, 0x00020000u};

  if (__builtin_expect(wave >= 4, 1)) {
    if (__builtin_expect(bid < NU, 1)) {
      const GAS char* gp = (const GAS char*)(c < 8 ? (isq ? wq : wg) : (isq ? wq : wu)) + uo;
      u32x4 a[18];
      u32x3 xr[3];
      unit_load<0, NAUX, 0>(a, gp, stride);
      load_x(mkrsrc(x, 2 * DM * 2), ((lane >> 5) * 288 + (lane & 31) * 3) * 4, xr);
      const u32 qe = __builtin_amdgcn_raw_buffer_load_b32(rw, WS_E, 0, AUX_SC1);
      SBAR();
      unit_load<NAUX, (2 * NAUX < 18 ? 2 * NAUX : 18), NAUX + 4>(a, gp, stride);
      unit_load<2 * NAUX, 18, NAUX>(a, gp, stride);
      STAMP2(24);
      const Sel sel = make_sel(lane);
      LAS u32* xn = lds + L_XN + aw * 576;
      layernorm(xr, xn, lane);
      u32 xm[9];
#pragma unroll
      for (int k = 0; k < 9; ++k) xm[k] = xn[((lane >> 1) & 1) * 288 + 144 * (bid & 1) + 36 * aw + 9 * g + k];
      STAMP2(1);
      f32x4 d = {0.f, 0.f, 0.f, 0.f};
#pragma unroll
      for (int k = 0; k < 18; ++k) d = mfma(a[k], build_b(xm[k >> 1], sel, k & 1), d);
      LAS float* red = (LAS float*)lds + L_RED;
#pragma unroll
      for (int i = 0; i < 4; ++i) red[aw * ACC_W + ((lane >> 1) & 1) * 176 + g * 40 + 8 * i + ((lane >> 2) & 3) * 2 + (lane & 1)] = d[i];
      lds_fence();
      STAMP2(2);
      u32 ticket = 0;
      if (lane == 0) ticket = __hip_atomic_fetch_add(lds + L_CTR, 1u, __ATOMIC_RELAXED, __HIP_MEMORY_SCOPE_WORKGROUP);
      if (__builtin_amdgcn_readfirstlane(ticket) == 3u) {
        asm volatile("" ::: "memory");
        const u32 tag = next_tag(__builtin_amdgcn_readfirstlane(qe));
        const LAS float* p = red + (lane >> 5) * 176 + ((lane >> 3) & 3) * 40 + (lane & 7) * 4;
        const f32x4 s = (*(const LAS f32x4*)p + *(const LAS f32x4*)(p + ACC_W)) + (*(const LAS f32x4*)(p + 2 * ACC_W) + *(const LAS f32x4*)(p + 3 * ACC_W));
        __builtin_amdgcn_raw_buffer_store_b128(pack4(__builtin_bit_cast(f32x4, __builtin_bit_cast(u32x4, s) + 0x80u), tag), rw, WS_PG + bid * 1024 + lane * 16, 0, AUX_SC1);
      }
      STAMP(6);
    }
    if ((bid < O0 || (bid >= Z0 && bid < Z0 + 8)) && aw < 2) {
      const int f = bid < O0 ? bid : bid - (Z0 - O0);
      const u32 oi = (lane >> 5) * DM + 8 * f + 2 * (lane & 3) + (g & 1);
      u32 xv = ((const GAS u16*)x)[oi];
      u32 op = (lane & 12) == 0 ? oi * 2 : OOB;
      u32 go = (aw ? WS_FG : WS_YG) + (f >> 1) * (NF * 128) + (lane >> 2) * 128 + (f & 1) * 64 + (lane & 3) * 16;
      asm volatile("" : "+v"(xv), "+v"(op), "+v"(go));
      wg_barrier();
      STAMPZ(31);
      const u32 tag = lds[L_TAG];
      LAS float* lg = (LAS float*)lds + L_RED + lane;
      const u32 t16 = __builtin_amdgcn_readfirstlane(tag) << 16;
      u32x4 r[5];
      if (aw) {
        gather<5>(r, rws, go, t16);
        *lg = rbf(silu(rbf(sum_parts<5>(r))));
        STAMPZ(7);
        wg_barrier();
        STAMPZ_OUT();
        return;
      }
      gather<4>(r, rws, go, t16);
      STAMP2(3);
      wg_barrier();
      float gt = *lg;
      SBAR();
      float sm = rbf(sum_parts<4>(r));
      asm volatile("" : "+v"(gt), "+v"(sm));
      const u32 res = pk(lo16(xv) + rbf(sm * gt), 0.f);
      __builtin_amdgcn_raw_buffer_store_b16((u16)res, mkrsrc(out, 2 * DM * 2), op, 0, 0);
      if (f == 0 && lane == 0) __builtin_amdgcn_raw_buffer_store_b32(tag, rw, WS_E, 0, AUX_SC1);
      STAMPZ(4);
      STAMPZ_OUT();
      return;
    }
    STAMP_OUT();
    return;
  }

  {
    const u32 ko = ((lane >> 2) & 3) * 256 + (4 * g + (lane & 3)) * 16;
    u32 cid = 0;
#pragma unroll
    for (int j = 0; j < 8; ++j) cid ^= ((bid >> j) & 1) ? MC[j] : 0u;
    const rsrc_t rk = mkrsrc(kc + (size_t)(cid >> 3) * PAIR, PAIR), rv = mkrsrc(vc + (size_t)(cid >> 3) * PAIR, PAIR);
    const u32 s0 = mapa<0, 10>(cid | (wave << 8));
    const bool odd = lane & 1;
    const u32 vo = lane * 16;
    u32x4 r0[8], r1[8], r2[8], r3[8], qg[2];
    u32 so = s0, e = 0, qo = 0;
#pragma unroll
    for (int p = 0; p < 32; ++p) {
      if (p == NPRE) { if (bid < NU) { lds[L_CTR] = 0u; wg_barrier(); } else __builtin_amdgcn_s_sleep(IDLE); SBAR(); }
      if (p == NPRO) STAMP2(24);
      if (p == NPRO) {
        u32 ln = lane; asm volatile("" : "+v"(ln));
        qo = WS_PG + ((cid >> 3) & 15) * 4096 + ((ln >> 1) & 1) * 2048 + (cid >> 7) * 512 + (ln >> 2) * 32 + (ln & 1) * 16;
      }
      if (p == QPOS) {
        e = __builtin_amdgcn_raw_buffer_load_b32(rw, WS_E, 0, AUX_SC1);
        qg[0] = ldsc(rw, qo, 0); qg[1] = ldsc(rw, qo, 1024);
      }
      if (p >= QPOS && p < QPOS + NPRO) pace<NPRO + 3>();
      else if (p >= NPRO) pace<NPRO>();
      if (p == NPRO) STAMP2(26);
      if (p == 8 + NPRO) STAMP2(28);
      if (p == 16 + NPRO) STAMP2(29);
      const u32x4 t = ldnt(rk, ko, so ^ mapa<10, 5>(p));
      if (p < 8) r0[p] = t; else if (p < 16) r1[p - 8] = t; else if (p < 24) r2[p - 16] = t; else r3[p - 24] = t;
      SBAR();
    }
    STAMP(1);
    const u32 tag = next_tag(__builtin_amdgcn_readfirstlane(e));
    if (!tags16_ok<2>(qg, tag)) {
      u32 spins = SPIN_CAP;
      _Pragma("nounroll") do { RELOAD(); qg[0] = ldsc(rw, qo, 0); qg[1] = ldsc(rw, qo, 1024); } while (!tags16_ok<2>(qg, tag) && --spins);
    }
    STAMP(2);
    Sched sc;
    sc.mask = 0xffu; sc.due = 0u; sc.on = 0u; sc.tag = tag; sc.prev = 0u; sc.dt = SCH_DT;
    sc.lead = (bid >= O0 && bid < F0) ? LEAD_O : ((bid >= F0 && bid < Z0) ? LEAD_F : 0);
    sc.ldo = lane < NS ? WS_SC + lane * SC_STR : OOB;
    sc.sample = bid == (NS == 16 ? 17 : 33) * (bid & (NS - 1)) && wave == (bid & 3);
    sc.sto = lane == 0 ? WS_SC + (bid & (NS - 1)) * SC_STR : OOB;

    LAS f32x4* sl = (LAS f32x4*)(lds + L_S) + wave * 512 + lane;
    float ml;
    {
      const f32x4 qf = unpack4(qg[0]) + unpack4(qg[1]);
      const u32 q0 = pk(qf[0], qf[1]), q1 = pk(qf[2], qf[3]), p0 = dppu<XOR1>(q0), p1 = dppu<XOR1>(q1);
      const u32x4 bq0 = {odd ? 0u : q0, odd ? 0u : q1, odd ? 0u : p0, odd ? 0u : p1}, bq1 = {odd ? p0 : 0u, odd ? p1 : 0u, odd ? q0 : 0u, odd ? q1 : 0u};
      u32x4 u = {0u, 0u, 0u, 0u};
      float mx = -INFINITY;
      for (int it = 0; it < 8; ++it) {
        int itv = it; asm volatile("" : "+s"(itv));
        const bool last = itv == 7;
        const rsrc_t r = mkrsrc((last ? vc : kc) + (size_t)(cid >> 3) * PAIR, PAIR);
        const u32 lo = last ? vo : ko;
        so = s0 ^ mapa<15, 3>(it + 1);
        f32x4 s4;
        s4[0] = k_tile<0>(r0, bq0, bq1, u, r, lo, so, sc, rw, it);
        s4[1] = k_tile<1>(r1, bq0, bq1, u, r, lo, so, sc, rw, it);
        s4[2] = k_tile<2>(r2, bq0, bq1, u, r, lo, so, sc, rw, it);
        s4[3] = k_tile<3>(r3, bq0, bq1, u, r, lo, so, sc, rw, it);
        sl[it * 64] = s4;
        mx = fmaxf(fmaxf(mx, fmaxf(s4[0], s4[1])), fmaxf(s4[2], s4[3]));
        STAMP(8 + it);
      }
      mx = fmaxf(mx, dppf<XOR1>(mx)); mx = fmaxf(mx, dppf<ROR4>(mx)); mx = fmaxf(mx, dppf<ROR8>(mx));
      ml = max32(max16(mx)) * LOG2E;
    }
    const u32 ho = WS_PG + (64 + 2 * (bid - F0) + ((lane >> 2) & 1)) * 1024 + ((lane >> 1) & 1) * 512 + (lane & 1) * 256 + (4 * wave + g) * 16;
    u32x4 hq = ldsc(rw, (bid >= F0 && bid < Z0) ? ho : OOB, 0);
    const bool isO = bid >= O0 && bid < F0, isF = bid >= F0 && bid < Z0;
    const int u = bid - (isF ? F0 : O0);
    const rsrc_t rl = mkrsrc(isF ? wd : wo, isF ? FF * DM * 2 : (isO ? HQ * DH * DM * 2 : 0));
    float l = 0.f;
    f32x4 acc = {0.f, 0.f, 0.f, 0.f};
    u32x4 bp0 = {0u, 0u, 0u, 0u}, bp1 = {0u, 0u, 0u, 0u};
    const u32 hm = odd ? 0xffff0000u : 0x0000ffffu;
    f32x4 s4 = sl[0];
    for (int it2 = 8; it2 < 15; ++it2) {
      int it = it2;
      asm volatile("" : "+s"(it));
      so = s0 ^ mapa<15, 3>(it + 1);
      const f32x4 sn = sl[(it - 7) * 64];
      v_tile<0>(r0, probs(s4[0], ml, l, hm), acc, bp0, bp1, rv, vo, so, sc, rw, it);
      v_tile<1>(r1, probs(s4[1], ml, l, hm), acc, bp0, bp1, rv, vo, so, sc, rw, it);
      v_tile<2>(r2, probs(s4[2], ml, l, hm), acc, bp0, bp1, rv, vo, so, sc, rw, it);
      v_tile<3>(r3, probs(s4[3], ml, l, hm), acc, bp0, bp1, rv, vo, so, sc, rw, it);
      s4 = sn;
      STAMP(8 + it);
    }
    const u32 wv = (64 * u + 16 * wave + 4 * g) * 1152 + c * 16, wv4 = c < 8 ? wv : OOB;
    u32x4 a[20];
    v_last<0, NE0, 0>(r0, probs(s4[0], ml, l, hm), acc, bp0, bp1, a, rl, wv, wv4);
    v_last<0, 0, 0>(r1, probs(s4[1], ml, l, hm), acc, bp0, bp1, a, rl, wv, wv4);
    v_last<0, 0, 0>(r2, probs(s4[2], ml, l, hm), acc, bp0, bp1, a, rl, wv, wv4);
    const u32x2 z3 = probs(s4[3], ml, l, hm);
    l += dppf<XOR1>(l); l += dppf<ROR4>(l); l += dppf<ROR8>(l); l = sum32(sum16(l));
    LAS float* lf = (LAS float*)lds;
    if (lane < 4) lf[L_ML + lane * 4 + wave] = odd ? l : ml;
    lds[L_TAG] = tag;
    u32 ad = (L_ACC + wave * ACC_W + ((lane >> 1) & 1) * 176 + g * 40 + ((lane >> 2) & 3) * 2 + (lane & 1)) * 4;
    u32 as = (L_ACC + (lane >> 5) * 176 + ((lane >> 3) & 3) * 40 + (lane & 7) * 4) * 4, am = (L_ML + (lane >> 5) * 8) * 4;
    u32 ao = WS_AG + (((cid >> 3) & 15) * 16 + (lane >> 2)) * 1024 + (((cid >> 1) & 3) * 4 + (cid >> 7) * 2 + (cid & 1)) * 64 + (lane & 3) * 16;
    asm volatile("" : "+v"(ao), "+v"(ad), "+v"(as), "+v"(am));
    SBAR();
    v_last<8 - NE1, NE1, NE0>(r3, z3, acc, bp0, bp1, a, rl, wv, wv4);
    STAMP2(3);
#pragma unroll
    for (int i = 0; i < 4; ++i) *(LAS float*)((LAS char*)lds + ad + 32 * i) = acc[i];
    wg_barrier();
    STAMP(4);
    if (wave == 0) {
      const f32x4 mw = *(const LAS f32x4*)((LAS char*)lds + am), lw = *(const LAS f32x4*)((LAS char*)lds + am + 16);
      f32x4 pa[4];
#pragma unroll
      for (int w = 0; w < 4; ++w) pa[w] = *(const LAS f32x4*)((LAS char*)lds + as + w * (ACC_W * 4));
      SBAR();
      const float M = fmaxf(fmaxf(mw[0], mw[1]), fmaxf(mw[2], mw[3]));
      const f32x4 ew = {ex2(mw[0] - M), ex2(mw[1] - M), ex2(mw[2] - M), ex2(mw[3] - M)};
      const float L = fmaf(ew[0], lw[0], ew[1] * lw[1]) + fmaf(ew[2], lw[2], ew[3] * lw[3]);
      const f32x4 o = (ew[0] * pa[0] + ew[1] * pa[1] + ew[2] * pa[2] + ew[3] * pa[3]) * rcp(L);
      const u32 lse = asu(M + __builtin_amdgcn_logf(L));
      stsc(rw, ao, u32x4{pk(o[0], o[1]), __builtin_amdgcn_perm(tag, lse, 0x05040100u), pk(o[2], o[3]), __builtin_amdgcn_perm(tag, lse, 0x05040302u)});
    }
    STAMP2(5);
    if (isO || isF) {
      rows_load<NE0 + NE1, 20>(a, rl, wv, wv4);
      Sel sel = make_sel(lane);
      u32 po = (isF ? WS_FG : WS_YG) + ((wave * 8 + (lane >> 3)) * NF + u) * 128 + (lane & 7) * 16, po2 = (wave == 0 && lane < 32) ? po + 32 * NF * 128 : OOB;
      u32 yd = L_Y * 4 + (wave * 400 + g * 20 + c) * 16, ys = L_Y * 4 + (wave * 80 + (lane & 3) * 20 + (lane >> 2)) * 16;
      pin_sel(sel);
      asm volatile("" : "+v"(po), "+v"(po2), "+v"(yd), "+v"(ys));
      STAMP(6);
      u32 m0, m1;
      if (isO) {
        const u32 go = WS_AG + (u * 4 + wave) * 1024 + c * 64 + g * 16;
        const u32x4 r = gather1(rws, go, tag << 16);
        STAMP2(7);
        const float lse = asf(__builtin_amdgcn_perm(r[3], r[1], 0x05040100u));
        float M = fmaxf(lse, dppf<XOR1>(lse)); M = fmaxf(M, dppf<ROR4>(M)); M = fmaxf(M, dppf<ROR8>(M));
        const float wt = ex2(lse - M);
        float o[5] = {wt * lo16(r[0]), wt * hi16(r[0]), wt * lo16(r[2]), wt * hi16(r[2]), wt};
#define RED5(C) _Pragma("unroll") for (int i = 0; i < 5; ++i) { o[i] += dppf<C>(o[i]); asm volatile("" : "+v"(o[i])); }
        asm volatile("" : "+v"(o[0]), "+v"(o[1]), "+v"(o[2]), "+v"(o[3]));
        RED5(XOR1) RED5(ROR4) RED5(ROR8)
#undef RED5
        const float inv = rcp(o[4]);
        m0 = pk(o[0] * inv, o[1] * inv); m1 = pk(o[2] * inv, o[3] * inv);
      } else {
        if (!tags16_ok<1>(&hq, tag)) hq = gather1(rws, ho, tag << 16);
        f32x4 h = unpack4(hq);
#pragma unroll
        for (int i = 0; i < 4; ++i) {
          const float own = rbf(h[i] + dppf<ROR4>(h[i])), oth = dppf<XOR1>(own);
          const float gt = odd ? oth : own, up = odd ? own : oth;
          h[i] = rbf(silu(gt)) * up;
        }
        m0 = pk(h[0], h[1]); m1 = pk(h[2], h[3]);
      }
      f32x4 y[5];
      const u32x4 b0 = build_b(m0, sel, 0), b1 = build_b(m0, sel, 1), b2 = build_b(m1, sel, 0), b3 = build_b(m1, sel, 1);
#pragma unroll
      for (int cb = 0; cb < 5; ++cb) y[cb] = mfma(b0, a[cb], f32x4{0.f, 0.f, 0.f, 0.f});
#pragma unroll
      for (int cb = 0; cb < 5; ++cb) y[cb] = mfma(b1, a[5 + cb], y[cb]);
      SBAR();
#pragma unroll
      for (int cb = 0; cb < 5; ++cb) y[cb] = mfma(b2, a[10 + cb], y[cb]);
      SBAR();
#pragma unroll
      for (int cb = 0; cb < 5; ++cb) { y[cb] = mfma(b3, a[15 + cb], y[cb]); SBAR(); }
#pragma unroll
      for (int cb = 0; cb < 5; ++cb) *(LAS f32x4*)((LAS char*)lds + yd + cb * 1280) = y[cb];
      wg_barrier();
      const LAS f32x4* p = (const LAS f32x4*)((LAS char*)lds + ys);
      const f32x4 t0 = p[0], t1 = p[400], t2 = p[800], t3 = p[1200], t4 = p[320], t5 = p[720], t6 = p[1120], t7 = p[1520];
      stsc(rw, po, pack4((t0 + t1) + (t2 + t3), tag));
      stsc(rw, po2, pack4((t4 + t5) + (t6 + t7), tag));
      STAMP(30);
    }
    STAMP_OUT();
    return;
  }
}

extern "C" void kernel_launch(void* const* d_in, const int* in_sizes, int n_in, void* d_out, int out_size, void* d_ws, size_t ws_size,
                              hipStream_t stream) {
  fused_decode<<<NWG, NTHR, 0, stream>>>((const u16*)d_in[1], (const u16*)d_in[2], (unsigned char*)d_ws, (const u16*)d_in[0], (const u16*)d_in[3],
                                          (const u16*)d_in[4], (const u16*)d_in[6], (const u16*)d_in[7], (const u16*)d_in[8], (u16*)d_out);
}
```

```cpp
#include <hip/hip_runtime.h>
#include <hip/hip_bf16.h>
#include <stdint.h>

typedef unsigned u32;
typedef unsigned short u16;
typedef u32 u32x2 __attribute__((ext_vector_type(2)));
typedef u32 u32x3 __attribute__((ext_vector_type(3)));
typedef u32 u32x4 __attribute__((ext_vector_type(4)));
typedef float f32x2 __attribute__((ext_vector_type(2)));
typedef float f32x4 __attribute__((ext_vector_type(4)));
typedef __bf16 bf16x8 __attribute__((ext_vector_type(8)));
typedef __bf16 bf16x2 __attribute__((ext_vector_type(2)));
typedef __amdgpu_buffer_rsrc_t rsrc_t;
#define GAS __attribute__((address_space(1)))
#define LAS __attribute__((address_space(3)))

#define NPRO 5
#define NFL 5
#define NHI 9
#define SCH_DT 211
#define SCH_DT0 205
#define SCH_SH 3
#define SCH_BIAS 4
#define NS 8
#define SC_S 4352
#define SC_MASK 0x4924
#define SCH_TAB 0x0011495557777FFFull
#define SCH_LOW 0x5555555549110100ull
#define LEAD_O 99
#define LEAD_F 76
#define QPOS 26
#define NAUX 9
#define PF0 208
#define NE1 5
#define NPRE 0
#define IDLE 56
#define RELEASE 0
#define MAPA 0xa4, 0x180, 0x21e4, 0x6194, 0x7730, 0x15bc, 0x1a28, 0x21a4, 0x2024, 0x90, 0x100, 0x2120, 0x7294, 0x3030, 0x4080, 0x1d5c, 0x508, 0x185c
#define MAPC 0x23, 0x8, 0x94, 0x1, 0x10, 0x2, 0x40, 0x80
#define SPG 3
#define SPI 2
#define ADT 2
#define BIAS_V -8
#define ROWP 1
#define NE0 1
#define NLATE 8
#define CODEPF_BYTES 18048
#define CODEPF 1

constexpr int DM = 576, HQ = 32, SK = 32768, DH = 128, FF = 4608;
constexpr int NWG = 256, NTHR = 512;
constexpr u32 CHUNK = 4096u * DH * 2u;
constexpr u32 PAIR = 8 * CHUNK;
constexpr u32 MA[18] = {MAPA}, MC[8] = {MAPC};
template <int LO, int N> __host__ __device__ constexpr u32 mapa(u32 v) { u32 r = 0; for (int j = 0; j < N; ++j) r ^= ((v >> j) & 1) ? MA[LO + j] << 8 : 0u; return r; }
constexpr int TPW = 32;
constexpr int AUX_NT = 2, AUX_SC1 = 16;
constexpr u32 OOB = 0x80000000u;
constexpr int NU = 208, O0 = 64, F0 = 128, NF = 72, Z0 = 200;
constexpr u32 WS_E = 0, WS_PG = 4096, WS_AG = WS_PG + NU * 1024, WS_YG = WS_AG + NWG * 1024, WS_FG = WS_YG + 36 * NF * 128,
              WS_SC = WS_FG + 36 * NF * 128, SC_STR = SC_S, SC_ROW = SC_S < 128 ? 4352 : NS * SC_S, WS_END = WS_SC + 16 * SC_ROW;
constexpr u32 SPIN_CAP = 400000;
constexpr int L_CTR = 0, L_TAG = 4, L_ML = 8, L_ACC = 32, ACC_W = 352, L_XN = L_ACC + 4 * ACC_W, L_RED = L_XN + 4 * 576, L_Y = L_RED + 4 * ACC_W,
              L_S = L_Y + 4 * 1600, L_END = L_S + 4 * 2048;
constexpr float LOG2E = 1.4426950408889634f;

#define STAMPZ(k)
#define STAMPZ_OUT()
#define STAMP(k)
#define STAMP_OUT()
#define STAMP2(k)

__device__ __forceinline__ rsrc_t mkrsrc(const void* p, u32 bytes) { return __builtin_amdgcn_make_buffer_rsrc((void*)p, 0, bytes, 0x00020000); }
__device__ __forceinline__ u32x4 ldnt(rsrc_t r, u32 vo, u32 so) { return __builtin_amdgcn_raw_buffer_load_b128(r, vo, so, AUX_NT); }
__device__ __forceinline__ u32x4 ldsc(rsrc_t r, u32 vo, u32 so) { return __builtin_amdgcn_raw_buffer_load_b128(r, vo, so, AUX_SC1); }
__device__ __forceinline__ void stsc(rsrc_t r, u32 vo, u32x4 v) { __builtin_amdgcn_raw_buffer_store_b128(v, r, vo, 0, AUX_SC1); }
__device__ __forceinline__ f32x4 mfma(u32x4 a, u32x4 b, f32x4 c) {
  return __builtin_amdgcn_mfma_f32_16x16x32_bf16(__builtin_bit_cast(bf16x8, a), __builtin_bit_cast(bf16x8, b), c, 0, 0, 0);
}
__device__ __forceinline__ float asf(u32 x) { return __builtin_bit_cast(float, x); }
__device__ __forceinline__ u32 asu(float x) { return __builtin_bit_cast(u32, x); }
__device__ __forceinline__ u32 pk(float lo, float hi) { bf16x2 v = {(__bf16)lo, (__bf16)hi}; return __builtin_bit_cast(u32, v); }
__device__ __forceinline__ float rbf(float x) { return (float)(__bf16)x; }
__device__ __forceinline__ float lo16(u32 x) { return asf(x << 16); }
__device__ __forceinline__ float hi16(u32 x) { return asf(x & 0xffff0000u); }
template <int CTRL> __device__ __forceinline__ u32 dppu(u32 x) { return (u32)__builtin_amdgcn_mov_dpp((int)x, CTRL, 0xf, 0xf, true); }
template <int CTRL> __device__ __forceinline__ float dppf(float x) { return asf(dppu<CTRL>(asu(x))); }
constexpr int XOR1 = 0xB1, XOR2 = 0x4E, XOR7 = 0x141, ROR4 = 0x124, ROR8 = 0x128;
__device__ __forceinline__ float sum16(float x) { auto s = __builtin_amdgcn_permlane16_swap(asu(x), asu(x), false, false); return asf(s[0]) + asf(s[1]); }
__device__ __forceinline__ float sum32(float x) { auto s = __builtin_amdgcn_permlane32_swap(asu(x), asu(x), false, false); return asf(s[0]) + asf(s[1]); }
__device__ __forceinline__ float max16(float x) { auto s = __builtin_amdgcn_permlane16_swap(asu(x), asu(x), false, false); return fmaxf(asf(s[0]), asf(s[1])); }
__device__ __forceinline__ float max32(float x) { auto s = __builtin_amdgcn_permlane32_swap(asu(x), asu(x), false, false); return fmaxf(asf(s[0]), asf(s[1])); }
__device__ __forceinline__ float ex2(float x) { return __builtin_amdgcn_exp2f(x); }
__device__ __forceinline__ float rcp(float x) { return __builtin_amdgcn_rcpf(x); }
__device__ __forceinline__ float silu(float x) { return x * rcp(1.0f + ex2(-LOG2E * x)); }
__device__ __forceinline__ u32 now32() { return (u32)__builtin_amdgcn_s_memrealtime(); }
template <int N> __device__ __forceinline__ void pace() { asm volatile("s_waitcnt vmcnt(%0)" ::"n"(N - 1) : "memory"); }
template <int BIT, int EXTRA> __device__ __forceinline__ void pacev(u32 mask) {
  asm volatile("s_bitcmp1_b32 %0, %4+8\n\ts_cbranch_scc0 1f\n\ts_waitcnt vmcnt(%3)\n1:\n\t"
               "s_bitcmp1_b32 %0, %4\n\ts_waitcnt vmcnt(%1)\n\ts_cbranch_scc0 2f\n\ts_waitcnt vmcnt(%2)\n2:"
               ::"s"(mask), "n"(NHI - 1 + EXTRA), "n"(NFL - 1 + EXTRA), "n"(NFL - 2 + EXTRA), "n"(BIT) : "memory", "scc");
}
__device__ __forceinline__ u32 pace_mask(u32 now, u32 due) {
  int up = (int)(now - due) >> SCH_SH, dn = -up;
  asm("s_max_i32 %0, %0, 0\n\ts_min_i32 %0, %0, 7\n\ts_max_i32 %1, %1, 0\n\ts_min_i32 %1, %1, 7" : "+s"(up), "+s"(dn) : : "scc");
  return ((u32)(SCH_TAB >> (8 * up)) & 0xffu) | ((u32)(SCH_LOW >> (8 * dn)) << 8);
}
#define SBAR() __builtin_amdgcn_sched_barrier(0)
#define RELOAD() asm volatile("" ::: "memory")
__device__ __forceinline__ void lds_fence() { asm volatile("s_waitcnt lgkmcnt(0)" ::: "memory"); }
__device__ __forceinline__ void wg_barrier() { lds_fence(); __builtin_amdgcn_s_barrier(); asm volatile("" ::: "memory"); }
__device__ __forceinline__ u32 next_tag(u32 e) { return e * 1664525u + 1013904223u; }

struct Sched { u32 mask, due, on, tag, lead, ldo, sto, prev, dt; bool sample; u32x2 row; };

template <int W, int T> __device__ __forceinline__ u32 bank_mov(u32 old, u32 z) {
  constexpr int ctrl = W == T ? 0xE4 : (W > T ? 0x110 + 4 * (W - T) : 0x100 + 4 * (T - W));
  return (u32)__builtin_amdgcn_update_dpp((int)old, (int)z, ctrl, 0xf, 1 << W, false);
}
template <int T> __device__ __forceinline__ void build_p(u32x4& b, u32 z) {
  b[0] = bank_mov<0, T>(b[0], z); b[1] = bank_mov<1, T>(b[1], z); b[2] = bank_mov<2, T>(b[2], z); b[3] = bank_mov<3, T>(b[3], z);
}

template <int GRP> __device__ __forceinline__ void sched_step(Sched& sc, u32 now, rsrc_t rw, int it) {
  if (GRP == 0) {
    sc.row = __builtin_amdgcn_raw_buffer_load_b64(rw, ((SC_MASK >> it) & 1) ? sc.ldo : OOB, it * SC_ROW, AUX_SC1);
    if (sc.sample && ((SC_MASK >> (it + 1)) & 1)) __builtin_amdgcn_raw_buffer_store_b64(u32x2{now + sc.lead, sc.tag}, rw, sc.sto, (it + 1) * SC_ROW, AUX_SC1);
  } else {
    const bool ok = __builtin_amdgcn_ballot_w64(sc.row[1] == sc.tag) == (1ull << NS) - 1;
    u32 d = sc.row[0] - now;
    d += dppu<XOR1>(d); d += dppu<XOR2>(d); d += dppu<XOR7>(d);
    if (NS == 16) d += dppu<ROR8>(d);
    const u32 mean = now + ((int)__builtin_amdgcn_readfirstlane(d) >> (NS == 16 ? 4 : 3)) - sc.lead;
    if (ok) {
      if (ADT && sc.on == (u32)it - 3u) {
        int m = (int)((mean - sc.prev) * 683u) >> 12;
        if (ADT == 2) m = (m + (int)sc.dt) >> 1;
        asm("s_max_i32 %0, %0, %1\n\ts_min_i32 %0, %0, %2" : "+s"(m) : "n"(SCH_DT - 8), "n"(SCH_DT + 24) : "scc");
        sc.dt = m;
      }
      sc.prev = mean; sc.on = it;
      sc.due = mean + 3 * sc.dt - (it >= 8 ? BIAS_V : SCH_BIAS);
    }
  }
  sc.mask = sc.on ? pace_mask(now, sc.due) : 0xffu;
  sc.due += sc.dt;
}
template <int GRP, int I>
__device__ __forceinline__ void refill(u32x4& slot, rsrc_t r, u32 vo, u32 so, Sched& sc, u32& now, rsrc_t rw, int it) {
  pacev<I, (GRP == 0 && I >= 2 && I < 2 + NFL) ? 1 : 0>(sc.mask);
  constexpr u32 step = mapa<10, 5>(GRP * 8 + I);
  slot = ldnt(r, vo, so ^ step);
  if (!(GRP & 1) && I == 0) now = now32();
  if (!(GRP & 1) && I == 1) sched_step<GRP>(sc, now, rw, it);
  if (SPG && GRP == SPG && I == SPI && it == 14 && sc.lead == 0) sc.mask = 0u;
  SBAR();
}
template <int T> __device__ __forceinline__ void k_pick(u32x4& u, f32x4 d) {
  u[0] = bank_mov<T, 0>(u[0], asu(d[0])); u[1] = bank_mov<T, 1>(u[1], asu(d[1])); u[2] = bank_mov<T, 2>(u[2], asu(d[2])); u[3] = bank_mov<T, 3>(u[3], asu(d[3]));
}
template <int GRP>
__device__ __forceinline__ float k_tile(u32x4 (&k)[8], u32x4 bq0, u32x4 bq1, u32x4& u, rsrc_t r, u32 vo, u32 so, Sched& sc, rsrc_t rw, int it) {
  const f32x4 z = {0.f, 0.f, 0.f, 0.f};
  f32x4 da, db;
  u32 now = 0;
#define RF(I) SBAR(); refill<GRP, I>(k[I], r, vo, so, sc, now, rw, it);
  da = mfma(k[0], bq0, z);                     RF(0)
  da = mfma(k[1], bq1, da);                    RF(1)
  db = mfma(k[2], bq0, z);                     RF(2)
  db = mfma(k[3], bq1, db); k_pick<0>(u, da);  RF(3)
  da = mfma(k[4], bq0, z);                     RF(4)
  da = mfma(k[5], bq1, da); k_pick<1>(u, db);  RF(5)
  db = mfma(k[6], bq0, z);                     RF(6)
  db = mfma(k[7], bq1, db); k_pick<2>(u, da);  RF(7)
#undef RF
  k_pick<3>(u, db);
  const float s = (asf(u[0]) + asf(u[1])) + (asf(u[2]) + asf(u[3]));
  return rbf(rbf(s) * 0.08838834764831845f);
}
__device__ __forceinline__ u32x2 probs(float s, float ml, float& l, u32 hm) {
  const float p = ex2(fmaf(s, LOG2E, -ml));
  l += p;
  const u32 t = pk(p, p);
  return u32x2{dppu<0xA0>(t) & hm, dppu<0xF5>(t) & hm};
}
template <int GRP>
__device__ __forceinline__ void v_tile(u32x4 (&v)[8], u32x2 z, f32x4& acc, u32x4& bp0, u32x4& bp1, rsrc_t r, u32 vo, u32 so, Sched& sc, rsrc_t rw, int it) {
  u32 now = 0;
#define PV_STEP(U, BP) build_p<(U) / 2>(BP, z[(U) & 1]); acc = mfma(v[U], BP, acc); SBAR(); refill<GRP, U>(v[U], r, vo, so, sc, now, rw, it);
  PV_STEP(0, bp0) PV_STEP(1, bp1) PV_STEP(2, bp0) PV_STEP(3, bp1) PV_STEP(4, bp0) PV_STEP(5, bp1) PV_STEP(6, bp0) PV_STEP(7, bp1)
#undef PV_STEP
}
template <int K> __device__ __forceinline__ u32x4 row_load(rsrc_t r, u32 vo, u32 vo4) { return ldnt(r, K % 5 == 4 ? vo4 : vo, (K / 5) * 1152 + (K % 5) * 256); }
template <int FROM, int CNT, int K0>
__device__ __forceinline__ void v_last(const u32x4 (&v)[8], u32x2 z, f32x4& acc, u32x4& bp0, u32x4& bp1, u32x4 (&a)[20], rsrc_t r, u32 vo, u32 vo4) {
#define PV_STEP(U, BP) build_p<(U) / 2>(BP, z[(U) & 1]); acc = mfma(v[U], BP, acc); SBAR(); \
  if constexpr (U >= FROM && U < FROM + CNT) { a[K0 + U - FROM] = row_load<K0 + U - FROM>(r, vo, vo4); SBAR(); }
  PV_STEP(0, bp0) PV_STEP(1, bp1) PV_STEP(2, bp0) PV_STEP(3, bp1) PV_STEP(4, bp0) PV_STEP(5, bp1) PV_STEP(6, bp0) PV_STEP(7, bp1)
#undef PV_STEP
}
template <int LO, int HI> __device__ __forceinline__ void rows_load(u32x4 (&a)[20], rsrc_t r, u32 vo, u32 vo4) {
  if constexpr (LO < HI) { pace<NLATE>(); a[LO] = row_load<LO>(r, vo, vo4); SBAR(); rows_load<LO + 1, HI>(a, r, vo, vo4); }
}

struct Sel { u32 s[4][2]; };
__device__ __forceinline__ Sel make_sel(int lane) {
  Sel r; const int wp = (lane >> 2) & 3; const bool hb = lane & 1;
#pragma unroll
  for (int par = 0; par < 2; ++par) {
    const u32 two = ((2 * par + 1) << 8) | (2 * par);
    const u32 base = hb ? ((two << 16) | 0x0c0cu) : (0x0c0c0000u | two);
#pragma unroll
    for (int w = 0; w < 4; ++w) r.s[w][par] = wp == w ? base : 0x0c0c0c0cu;
  }
  return r;
}
__device__ __forceinline__ void pin_sel(Sel& s) {
  asm volatile("" : "+v"(s.s[0][0]), "+v"(s.s[0][1]), "+v"(s.s[1][0]), "+v"(s.s[1][1]), "+v"(s.s[2][0]), "+v"(s.s[2][1]), "+v"(s.s[3][0]), "+v"(s.s[3][1]));
}
__device__ __forceinline__ u32x4 build_b(u32 d, const Sel& sel, int par) {
  u32x4 b;
#pragma unroll
  for (int w = 0; w < 4; ++w) b[w] = __builtin_amdgcn_perm(d, d, sel.s[w][par]);
  return b;
}
__device__ __forceinline__ void load_x(rsrc_t rx, u32 xo, u32x3 (&xr)[3]) {
#pragma unroll
  for (int k = 0; k < 3; ++k) xr[k] = __builtin_amdgcn_raw_buffer_load_b96(rx, xo, k * 384, 0);
}
template <int LO, int HI, int N> __device__ __forceinline__ void unit_load(u32x4 (&a)[18], rsrc_t r, u32 vo, u32 stride) {
  if constexpr (LO < HI) {
    if constexpr (N > 0) pace<N>();
    a[LO] = ldnt(r, vo, (ROWP ? 32 * (LO >> 1) + (LO & 1) : LO) * stride);
    if constexpr (LO == 17 - RELEASE) __builtin_amdgcn_s_barrier();
    SBAR();
    unit_load<LO + 1, HI, N>(a, r, vo, stride);
  }
}
__device__ __forceinline__ void layernorm(const u32x3 (&xr)[3], LAS u32* xn, int lane) {
  f32x2 v[9], s2 = {0.f, 0.f};
#pragma unroll
  for (int k = 0; k < 9; ++k) { v[k] = f32x2{lo16(xr[k / 3][k % 3]), hi16(xr[k / 3][k % 3])}; s2 += v[k]; }
  float s = s2[0] + s2[1];
  s += dppf<XOR1>(s); s += dppf<XOR2>(s); s += dppf<ROR4>(s); s += dppf<ROR8>(s); s = sum16(s);
  const float mean = s * (1.0f / DM);
  f32x2 q2 = {0.f, 0.f};
#pragma unroll
  for (int k = 0; k < 9; ++k) { const f32x2 a = v[k] - mean; q2 += a * a; }
  float q = q2[0] + q2[1];
  q += dppf<XOR1>(q); q += dppf<XOR2>(q); q += dppf<ROR4>(q); q += dppf<ROR8>(q); q = sum16(q);
  const float mb = rbf(mean), den = rbf(__builtin_amdgcn_sqrtf(rbf(rbf(q * (1.0f / DM)) + 1e-5f))), inv = rcp(den);
#pragma unroll
  for (int k = 0; k < 9; ++k) {
    const f32x2 a = v[k] - mb;
    const u32 r = pk(a[0], a[1]);
    const f32x2 b = f32x2{lo16(r), hi16(r)} * inv;
    xn[(lane >> 5) * 288 + (lane & 31) * 3 + 96 * (k / 3) + k % 3] = pk(b[0], b[1]);
  }
  lds_fence();
}
__device__ __forceinline__ u32x4 pack4(f32x4 v, u32 tag) {
  return u32x4{__builtin_amdgcn_perm(asu(v[1]), asu(v[0]), 0x05030201u), __builtin_amdgcn_perm(tag, asu(v[1]), 0x05040302u),
               __builtin_amdgcn_perm(asu(v[3]), asu(v[2]), 0x05030201u), __builtin_amdgcn_perm(tag, asu(v[3]), 0x05040302u)};
}
__device__ __forceinline__ f32x4 unpack4(u32x4 r) {
  return f32x4{asf(r[0] << 8), asf(__builtin_amdgcn_perm(r[1], r[0], 0x0504030cu)), asf(r[2] << 8), asf(__builtin_amdgcn_perm(r[3], r[2], 0x0504030cu))};
}
template <int N> __device__ __forceinline__ bool tags16_ok(const u32x4* r, u32 tag) {
  u32 bad = 0;
#pragma unroll
  for (int i = 0; i < N; ++i) bad |= (r[i][1] ^ (tag << 16)) | (r[i][3] ^ (tag << 16));
  return __builtin_amdgcn_ballot_w64(bad > 0xffffu) == 0ull;
}
typedef unsigned long long u64;
#define G_LD(M, R, SO, OFF) "s_wqm_b64 exec, " M "\n\tbuffer_load_dwordx4 " R ", %[vo], %[rs], " SO " offen " OFF " sc1\n\t"
#define G_CK(M, A, B) "v_bitop3_b32 %[t], " A ", " B ", %[tg] bitop3:0x7e\n\tv_cmp_lt_u32_e64 " M ", %[lim], %[t]\n\t"
#define G_END "s_cbranch_scc0 2f\n\ts_sub_u32 %[n], %[n], 1\n\ts_cbranch_scc0 1b\n2:"
__device__ __forceinline__ u32x4 gather1(u32x4 rs, u32 vo, u32 t16) {
  u32x4 r; u64 m0; u32 t, n = SPIN_CAP;
  asm volatile("1:\n\tbuffer_load_dwordx4 v[140:143], %[vo], %[rs], 0 offen sc1\n\ts_waitcnt vmcnt(0)\n\t" G_CK("%[m0]", "v141", "v143")
               "s_cmp_lg_u64 %[m0], 0\n\t" G_END
               : "=&{v[140:143]}"(r), [m0] "=&s"(m0), [t] "=&v"(t), [n] "+s"(n) : [vo] "v"(vo), [rs] "s"(rs), [tg] "s"(t16), [lim] "v"(0xffffu) : "memory", "scc");
  return r;
}
template <int N> __device__ __forceinline__ void gather(u32x4 (&r)[5], u32x4 rs, u32 vo, u32 t16) {
  u64 m0 = ~0ull, m1 = ~0ull, m2 = ~0ull, m3 = ~0ull, m4 = N > 4 ? 0xffffffffull : 0ull, any; u32 t, n = SPIN_CAP;
  r[4] = u32x4{0u, t16, 0u, t16};
  asm volatile("1:\n\t" G_LD("%[m0]", "v[40:43]", "0", "") G_LD("%[m1]", "v[44:47]", "0", "offset:2048") G_LD("%[m2]", "v[48:51]", "%[s4]", "")
               G_LD("%[m3]", "v[52:55]", "%[s4]", "offset:2048") ".if %[five]\n\t" G_LD("%[m4]", "v[56:59]", "%[s8]", "") ".endif\n\t"
               "s_mov_b64 exec, -1\n\ts_waitcnt vmcnt(0)\n\t"
               G_CK("%[m0]", "v41", "v43") G_CK("%[m1]", "v45", "v47") G_CK("%[m2]", "v49", "v51") G_CK("%[m3]", "v53", "v55")
               ".if %[five]\n\t" G_CK("%[m4]", "v57", "v59") ".endif\n\t"
               "s_or_b64 %[any], %[m0], %[m1]\n\ts_or_b64 %[any], %[any], %[m2]\n\ts_or_b64 %[any], %[any], %[m3]\n\t"
               ".if %[five]\n\ts_or_b64 %[any], %[any], %[m4]\n\t.endif\n\t" G_END
               : "=&{v[40:43]}"(r[0]), "=&{v[44:47]}"(r[1]), "=&{v[48:51]}"(r[2]), "=&{v[52:55]}"(r[3]), "+{v[56:59]}"(r[4]), [m0] "+s"(m0), [m1] "+s"(m1),
                 [m2] "+s"(m2), [m3] "+s"(m3), [m4] "+s"(m4), [any] "=&s"(any), [t] "=&v"(t), [n] "+s"(n)
               : [vo] "v"(vo), [rs] "s"(rs), [tg] "s"(t16), [lim] "v"(0xffffu), [s4] "s"(4096u), [s8] "s"(8192u), [five] "n"(N > 4) : "memory", "scc");
}
template <int N> __device__ __forceinline__ float sum_parts(const u32x4* r) {
  f32x4 a = unpack4(r[0]);
#pragma unroll
  for (int i = 1; i < N; ++i) a += unpack4(r[i]);
  const auto s0 = __builtin_amdgcn_permlane32_swap(asu(a[0]), asu(a[2]), false, false), s1 = __builtin_amdgcn_permlane32_swap(asu(a[1]), asu(a[3]), false, false);
  const auto s2 = __builtin_amdgcn_permlane16_swap(asu(asf(s0[0]) + asf(s0[1])), asu(asf(s1[0]) + asf(s1[1])), false, false);
  float t = asf(s2[0]) + asf(s2[1]);
  t += dppf<ROR4>(t); t += dppf<ROR8>(t);
  return t;
}

typedef u32 u32x16 __attribute__((ext_vector_type(16)));
__device__ __forceinline__ const char* mkptr(u32 lo, u32 hi) { return (const char*)(GAS char*)(((uint64_t)hi << 32) | lo); }

__global__ void __launch_bounds__(NTHR, 2) __attribute__((aligned(4096)))
fused_decode(const u16* __restrict__, const u16* __restrict__, unsigned char*, const u16* __restrict__, const u16* __restrict__,
             const u16* __restrict__, const u16* __restrict__, const u16* __restrict__, const u16* __restrict__, u16*) {
  __shared__ __attribute__((aligned(16))) u32 smem[L_END];
  LAS u32* lds = (LAS u32*)smem;
  u32x16 ar; u32x4 ar2;
  asm volatile("s_load_dwordx16 %0, %2, 0x0\n\ts_load_dwordx4 %1, %2, 0x40" : "=&s"(ar), "=&s"(ar2) : "s"(__builtin_amdgcn_kernarg_segment_ptr()));
  SBAR();
  const int lane = threadIdx.x & 63, wave = __builtin_amdgcn_readfirstlane(threadIdx.x >> 6), bid = blockIdx.x;
  {
    uint64_t pc;
    asm volatile("s_cmp_lt_u32 %1, 3\n\ts_cbranch_scc0 1f\n\t"
                 "s_getpc_b64 %0\n\tv_mbcnt_lo_u32_b32 v1, -1, 0\n\tv_mbcnt_hi_u32_b32 v1, -1, v1\n\tv_lshl_add_u32 v1, %1, 6, v1\n\t"
                 "v_lshlrev_b32 v1, 7, v1\n\tv_min_u32 v1, %2, v1\n\tglobal_load_dword v1, v1, %0\n\ts_waitcnt vmcnt(0)\n1:"
                 : "=&s"(pc) : "s"((u32)(wave - 4) | ((u32)((bid >> 4) - (PF0 >> 4)) << 4)), "n"(CODEPF_BYTES) : "scc", "v1");
  }
  const int g = lane >> 4, c = lane & 15, aw = wave - 4;
  const u32 stride = bid < 64 ? HQ * DH * 2 : FF * 2;
  u32 uo = (288 * (bid & 1) + (ROWP ? 8 * aw + 2 * g : 72 * aw + 18 * g)) * stride + ((bid >> 1) - (bid < 64 ? 0 : (bid < 136 ? 32 : 68))) * 256 + c * 16;
  asm volatile("" : "+v"(uo));
  SBAR();
  asm volatile("s_waitcnt lgkmcnt(0)" : "+s"(ar), "+s"(ar2));
  const char *kc = mkptr(ar[0], ar[1]), *vc = mkptr(ar[2], ar[3]), *x = mkptr(ar[6], ar[7]), *wq = mkptr(ar[8], ar[9]), *wo = mkptr(ar[10], ar[11]),
             *wg = mkptr(ar[12], ar[13]), *wu = mkptr(ar[14], ar[15]), *wd = mkptr(ar2[0], ar2[1]);
  char *ws = (char*)mkptr(ar[4], ar[5]), *out = (char*)mkptr(ar2[2], ar2[3]);
  const rsrc_t rw = mkrsrc(ws, WS_END);
  const u32x4 rws = {ar[4], ar[5] & 0xffffu, WS_END, 0x00020000u};

  if (__builtin_expect(wave >= 4, 1)) {
    if (__builtin_expect(bid < NU, 1)) {
      const rsrc_t ru = mkrsrc(bid < 64 ? wq : (bid < 136 ? wg : wu), DM * stride);
      u32x4 a[18];
      u32x3 xr[3];
      unit_load<0, NAUX, 0>(a, ru, uo, stride);
      load_x(mkrsrc(x, 2 * DM * 2), ((lane >> 5) * 288 + (lane & 31) * 3) * 4, xr);
      const u32 qe = __builtin_amdgcn_raw_buffer_load_b32(rw, WS_E, 0, AUX_SC1);
      SBAR();
      unit_load<NAUX, (2 * NAUX < 18 ? 2 * NAUX : 18), NAUX + 4>(a, ru, uo, stride);
      unit_load<2 * NAUX, 18, NAUX>(a, ru, uo, stride);
      STAMP2(24);
      const Sel sel = make_sel(lane);
      LAS u32* xn = lds + L_XN + aw * 576;
      layernorm(xr, xn, lane);
      u32 xm[9];
#pragma unroll
      for (int k = 0; k < 9; ++k) xm[k] = xn[((lane >> 1) & 1) * 288 + 144 * (bid & 1) + (ROWP ? 4 * aw + g + 16 * k : 36 * aw + 9 * g + k)];
      STAMP2(1);
      f32x4 d = {0.f, 0.f, 0.f, 0.f};
#pragma unroll
      for (int k = 0; k < 18; ++k) d = mfma(a[k], build_b(xm[k >> 1], sel, k & 1), d);
      LAS float* red = (LAS float*)lds + L_RED;
#pragma unroll
      for (int i = 0; i < 4; ++i) red[aw * ACC_W + ((lane >> 1) & 1) * 176 + g * 40 + 8 * i + ((lane >> 2) & 3) * 2 + (lane & 1)] = d[i];
      lds_fence();
      STAMP2(2);
      u32 ticket = 0;
      if (lane == 0) ticket = __hip_atomic_fetch_add(lds + L_CTR, 1u, __ATOMIC_RELAXED, __HIP_MEMORY_SCOPE_WORKGROUP);
      if (__builtin_amdgcn_readfirstlane(ticket) == 3u) {
        asm volatile("" ::: "memory");
        const u32 tag = next_tag(__builtin_amdgcn_readfirstlane(qe));
        const LAS float* p = red + (lane >> 5) * 176 + ((lane >> 3) & 3) * 40 + (lane & 7) * 4;
        const f32x4 s = (*(const LAS f32x4*)p + *(const LAS f32x4*)(p + ACC_W)) + (*(const LAS f32x4*)(p + 2 * ACC_W) + *(const LAS f32x4*)(p + 3 * ACC_W));
        __builtin_amdgcn_raw_buffer_store_b128(pack4(__builtin_bit_cast(f32x4, __builtin_bit_cast(u32x4, s) + 0x80u), tag), rw, WS_PG + bid * 1024 + lane * 16, 0, AUX_SC1);
      }
      STAMP(6);
    }
    if ((bid < O0 || (bid >= Z0 && bid < Z0 + 8)) && aw < 2) {
      const int f = bid < O0 ? bid : bid - (Z0 - O0);
      const u32 oi = (lane >> 5) * DM + 8 * f + 2 * (lane & 3) + (g & 1);
      u32 xv = ((const GAS u16*)x)[oi];
      u32 op = (lane & 12) == 0 ? oi * 2 : OOB;
      u32 go = (aw ? WS_FG : WS_YG) + (f >> 1) * (NF * 128) + (lane >> 2) * 128 + (f & 1) * 64 + (lane & 3) * 16;
      asm volatile("" : "+v"(xv), "+v"(op), "+v"(go));
      wg_barrier();
      STAMPZ(31);
      const u32 tag = lds[L_TAG];
      LAS float* lg = (LAS float*)lds + L_RED + lane;
      const u32 t16 = __builtin_amdgcn_readfirstlane(tag) << 16;
      u32x4 r[5];
      if (aw) {
        gather<5>(r, rws, go, t16);
        *lg = rbf(silu(rbf(sum_parts<5>(r))));
        STAMPZ(7);
        wg_barrier();
        STAMPZ_OUT();
        return;
      }
      gather<4>(r, rws, go, t16);
      STAMP2(3);
      wg_barrier();
      float gt = *lg;
      SBAR();
      float sm = rbf(sum_parts<4>(r));
      asm volatile("" : "+v"(gt), "+v"(sm));
      const u32 res = pk(lo16(xv) + rbf(sm * gt), 0.f);
      __builtin_amdgcn_raw_buffer_store_b16((u16)res, mkrsrc(out, 2 * DM * 2), op, 0, 0);
      if (f == 0 && lane == 0) __builtin_amdgcn_raw_buffer_store_b32(tag, rw, WS_E, 0, AUX_SC1);
      STAMPZ(4);
      STAMPZ_OUT();
      return;
    }
    STAMP_OUT();
    return;
  }

  {
    const u32 ko = ((lane >> 2) & 3) * 256 + (4 * g + (lane & 3)) * 16;
    u32 cid = 0;
#pragma unroll
    for (int j = 0; j < 8; ++j) cid ^= ((bid >> j) & 1) ? MC[j] : 0u;
    const rsrc_t rk = mkrsrc(kc + (size_t)(cid >> 3) * PAIR, PAIR), rv = mkrsrc(vc + (size_t)(cid >> 3) * PAIR, PAIR);
    const u32 s0 = mapa<0, 10>(cid | (wave << 8));
    const bool odd = lane & 1;
    const u32 vo = lane * 16;
    u32x4 r0[8], r1[8], r2[8], r3[8], qg[2];
    u32 so = s0, e = 0, qo = 0;
#pragma unroll
    for (int p = 0; p < 32; ++p) {
      if (p == NPRE) { if (bid < NU) { lds[L_CTR] = 0u; wg_barrier(); } else __builtin_amdgcn_s_sleep(IDLE); SBAR(); }
      if (p == NPRO) STAMP2(24);
      if (p == NPRO) {
        u32 ln = lane; asm volatile("" : "+v"(ln));
        qo = WS_PG + ((cid >> 3) & 15) * 4096 + ((ln >> 1) & 1) * 2048 + (cid >> 7) * 512 + (ln >> 2) * 32 + (ln & 1) * 16;
      }
      if (p == QPOS) {
        e = __builtin_amdgcn_raw_buffer_load_b32(rw, WS_E, 0, AUX_SC1);
        qg[0] = ldsc(rw, qo, 0); qg[1] = ldsc(rw, qo, 1024);
      }
      if (p >= QPOS && p < QPOS + NPRO) pace<NPRO + 3>();
      else if (p >= NPRO) pace<NPRO>();
      if (p == NPRO) STAMP2(26);
      if (p == 8 + NPRO) STAMP2(28);
      if (p == 16 + NPRO) STAMP2(29);
      const u32x4 t = ldnt(rk, ko, so ^ mapa<10, 5>(p));
      if (p < 8) r0[p] = t; else if (p < 16) r1[p - 8] = t; else if (p < 24) r2[p - 16] = t; else r3[p - 24] = t;
      SBAR();
    }
    STAMP(1);
    const u32 tag = next_tag(__builtin_amdgcn_readfirstlane(e));
    if (!tags16_ok<2>(qg, tag)) {
      u32 spins = SPIN_CAP;
      _Pragma("nounroll") do { RELOAD(); qg[0] = ldsc(rw, qo, 0); qg[1] = ldsc(rw, qo, 1024); } while (!tags16_ok<2>(qg, tag) && --spins);
    }
    STAMP(2);
    Sched sc;
    sc.mask = 0xffu; sc.due = 0u; sc.on = 0u; sc.tag = tag; sc.prev = 0u; sc.dt = SCH_DT0;
    sc.lead = (bid >= O0 && bid < F0) ? LEAD_O : ((bid >= F0 && bid < Z0) ? LEAD_F : 0);
    sc.ldo = lane < NS ? WS_SC + lane * SC_STR : OOB;
    sc.sample = bid == (NS == 16 ? 17 : 33) * (bid & (NS - 1)) && wave == (bid & 3);
    sc.sto = lane == 0 ? WS_SC + (bid & (NS - 1)) * SC_STR : OOB;

    LAS f32x4* sl = (LAS f32x4*)(lds + L_S) + wave * 512 + lane;
    float ml;
    {
      const f32x4 qf = unpack4(qg[0]) + unpack4(qg[1]);
      const u32 q0 = pk(qf[0], qf[1]), q1 = pk(qf[2], qf[3]), p0 = dppu<XOR1>(q0), p1 = dppu<XOR1>(q1);
      const u32x4 bq0 = {odd ? 0u : q0, odd ? 0u : q1, odd ? 0u : p0, odd ? 0u : p1}, bq1 = {odd ? p0 : 0u, odd ? p1 : 0u, odd ? q0 : 0u, odd ? q1 : 0u};
      u32x4 u = {0u, 0u, 0u, 0u};
      float mx = -INFINITY;
      for (int it = 0; it < 8; ++it) {
        int itv = it; asm volatile("" : "+s"(itv));
        const bool last = itv == 7;
        const rsrc_t r = mkrsrc((last ? vc : kc) + (size_t)(cid >> 3) * PAIR, PAIR);
        const u32 lo = last ? vo : ko;
        so = s0 ^ mapa<15, 3>(it + 1);
        f32x4 s4;
        s4[0] = k_tile<0>(r0, bq0, bq1, u, r, lo, so, sc, rw, it);
        s4[1] = k_tile<1>(r1, bq0, bq1, u, r, lo, so, sc, rw, it);
        s4[2] = k_tile<2>(r2, bq0, bq1, u, r, lo, so, sc, rw, it);
        s4[3] = k_tile<3>(r3, bq0, bq1, u, r, lo, so, sc, rw, it);
        sl[it * 64] = s4;
        mx = fmaxf(fmaxf(mx, fmaxf(s4[0], s4[1])), fmaxf(s4[2], s4[3]));
        STAMP(8 + it);
      }
      mx = fmaxf(mx, dppf<XOR1>(mx)); mx = fmaxf(mx, dppf<ROR4>(mx)); mx = fmaxf(mx, dppf<ROR8>(mx));
      ml = max32(max16(mx)) * LOG2E;
    }
    const u32 ho = WS_PG + (64 + 72 * (lane & 1) + ((bid - F0) & ~1) + ((lane >> 2) & 1)) * 1024 + ((lane >> 1) & 1) * 512 + (bid & 1) * 256 + (4 * wave + g) * 16;
    u32x4 hq = ldsc(rw, (bid >= F0 && bid < Z0) ? ho : OOB, 0);
    const bool isO = bid >= O0 && bid < F0, isF = bid >= F0 && bid < Z0;
    const int u = bid - (isF ? F0 : O0);
    const rsrc_t rl = mkrsrc(isF ? wd : wo, isF ? FF * DM * 2 : (isO ? HQ * DH * DM * 2 : 0));
    float l = 0.f;
    f32x4 acc = {0.f, 0.f, 0.f, 0.f};
    u32x4 bp0 = {0u, 0u, 0u, 0u}, bp1 = {0u, 0u, 0u, 0u};
    const u32 hm = odd ? 0xffff0000u : 0x0000ffffu;
    f32x4 s4 = sl[0];
    for (int it2 = 8; it2 < 15; ++it2) {
      int it = it2;
      asm volatile("" : "+s"(it));
      so = s0 ^ mapa<15, 3>(it + 1);
      const f32x4 sn = sl[(it - 7) * 64];
      v_tile<0>(r0, probs(s4[0], ml, l, hm), acc, bp0, bp1, rv, vo, so, sc, rw, it);
      v_tile<1>(r1, probs(s4[1], ml, l, hm), acc, bp0, bp1, rv, vo, so, sc, rw, it);
      v_tile<2>(r2, probs(s4[2], ml, l, hm), acc, bp0, bp1, rv, vo, so, sc, rw, it);
      v_tile<3>(r3, probs(s4[3], ml, l, hm), acc, bp0, bp1, rv, vo, so, sc, rw, it);
      s4 = sn;
      STAMP(8 + it);
    }
    const u32 wv = (64 * u + 16 * wave + 4 * g) * 1152 + c * 16, wv4 = c < 8 ? wv : OOB;
    u32x4 a[20];
    v_last<0, NE0, 0>(r0, probs(s4[0], ml, l, hm), acc, bp0, bp1, a, rl, wv, wv4);
    v_last<0, 0, 0>(r1, probs(s4[1], ml, l, hm), acc, bp0, bp1, a, rl, wv, wv4);
    v_last<0, 0, 0>(r2, probs(s4[2], ml, l, hm), acc, bp0, bp1, a, rl, wv, wv4);
    const u32x2 z3 = probs(s4[3], ml, l, hm);
    l += dppf<XOR1>(l); l += dppf<ROR4>(l); l += dppf<ROR8>(l); l = sum32(sum16(l));
    LAS float* lf = (LAS float*)lds;
    if (lane < 4) lf[L_ML + lane * 4 + wave] = odd ? l : ml;
    lds[L_TAG] = tag;
    u32 ad = (L_ACC + wave * ACC_W + ((lane >> 1) & 1) * 176 + g * 40 + ((lane >> 2) & 3) * 2 + (lane & 1)) * 4;
    u32 as = (L_ACC + (lane >> 5) * 176 + ((lane >> 3) & 3) * 40 + (lane & 7) * 4) * 4, am = (L_ML + (lane >> 5) * 8) * 4;
    u32 ao = WS_AG + (((cid >> 3) & 15) * 16 + (lane >> 2)) * 1024 + (((cid >> 1) & 3) * 4 + (cid >> 7) * 2 + (cid & 1)) * 64 + (lane & 3) * 16;
    asm volatile("" : "+v"(ao), "+v"(ad), "+v"(as), "+v"(am));
    SBAR();
    v_last<8 - NE1, NE1, NE0>(r3, z3, acc, bp0, bp1, a, rl, wv, wv4);
    STAMP2(3);
#pragma unroll
    for (int i = 0; i < 4; ++i) *(LAS float*)((LAS char*)lds + ad + 32 * i) = acc[i];
    wg_barrier();
    STAMP(4);
    if (wave == 0) {
      const f32x4 mw = *(const LAS f32x4*)((LAS char*)lds + am), lw = *(const LAS f32x4*)((LAS char*)lds + am + 16);
      f32x4 pa[4];
#pragma unroll
      for (int w = 0; w < 4; ++w) pa[w] = *(const LAS f32x4*)((LAS char*)lds + as + w * (ACC_W * 4));
      SBAR();
      const float M = fmaxf(fmaxf(mw[0], mw[1]), fmaxf(mw[2], mw[3]));
      const f32x4 ew = {ex2(mw[0] - M), ex2(mw[1] - M), ex2(mw[2] - M), ex2(mw[3] - M)};
      const float L = fmaf(ew[0], lw[0], ew[1] * lw[1]) + fmaf(ew[2], lw[2], ew[3] * lw[3]);
      const f32x4 o = (ew[0] * pa[0] + ew[1] * pa[1] + ew[2] * pa[2] + ew[3] * pa[3]) * rcp(L);
      const u32 lse = asu(M + __builtin_amdgcn_logf(L));
      stsc(rw, ao, u32x4{pk(o[0], o[1]), __builtin_amdgcn_perm(tag, lse, 0x05040100u), pk(o[2], o[3]), __builtin_amdgcn_perm(tag, lse, 0x05040302u)});
    }
    STAMP2(5);
    if (isO || isF) {
      rows_load<NE0 + NE1, 20>(a, rl, wv, wv4);
      Sel sel = make_sel(lane);
      u32 po = (isF ? WS_FG : WS_YG) + ((wave * 8 + (lane >> 3)) * NF + u) * 128 + (lane & 7) * 16, po2 = (wave == 0 && lane < 32) ? po + 32 * NF * 128 : OOB;
      u32 yd = L_Y * 4 + (wave * 400 + g * 20 + c) * 16, ys = L_Y * 4 + (wave * 80 + (lane & 3) * 20 + (lane >> 2)) * 16;
      pin_sel(sel);
      asm volatile("" : "+v"(po), "+v"(po2), "+v"(yd), "+v"(ys));
      STAMP(6);
      u32 m0, m1;
      if (isO) {
        const u32 go = WS_AG + (u * 4 + wave) * 1024 + c * 64 + g * 16;
        const u32x4 r = gather1(rws, go, tag << 16);
        STAMP2(7);
        const float lse = asf(__builtin_amdgcn_perm(r[3], r[1], 0x05040100u));
        float M = fmaxf(lse, dppf<XOR1>(lse)); M = fmaxf(M, dppf<ROR4>(M)); M = fmaxf(M, dppf<ROR8>(M));
        const float wt = ex2(lse - M);
        float o[5] = {wt * lo16(r[0]), wt * hi16(r[0]), wt * lo16(r[2]), wt * hi16(r[2]), wt};
#define RED5(C) _Pragma("unroll") for (int i = 0; i < 5; ++i) { o[i] += dppf<C>(o[i]); asm volatile("" : "+v"(o[i])); }
        asm volatile("" : "+v"(o[0]), "+v"(o[1]), "+v"(o[2]), "+v"(o[3]));
        RED5(XOR1) RED5(ROR4) RED5(ROR8)
#undef RED5
        const float inv = rcp(o[4]);
        m0 = pk(o[0] * inv, o[1] * inv); m1 = pk(o[2] * inv, o[3] * inv);
      } else {
        if (!tags16_ok<1>(&hq, tag)) hq = gather1(rws, ho, tag << 16);
        f32x4 h = unpack4(hq);
#pragma unroll
        for (int i = 0; i < 4; ++i) {
          const float own = rbf(h[i] + dppf<ROR4>(h[i])), oth = dppf<XOR1>(own);
          const float gt = odd ? oth : own, up = odd ? own : oth;
          h[i] = rbf(silu(gt)) * up;
        }
        m0 = pk(h[0], h[1]); m1 = pk(h[2], h[3]);
      }
      f32x4 y[5];
      const u32x4 b0 = build_b(m0, sel, 0), b1 = build_b(m0, sel, 1), b2 = build_b(m1, sel, 0), b3 = build_b(m1, sel, 1);
#pragma unroll
      for (int cb = 0; cb < 5; ++cb) y[cb] = mfma(b0, a[cb], f32x4{0.f, 0.f, 0.f, 0.f});
#pragma unroll
      for (int cb = 0; cb < 5; ++cb) y[cb] = mfma(b1, a[5 + cb], y[cb]);
      SBAR();
#pragma unroll
      for (int cb = 0; cb < 5; ++cb) y[cb] = mfma(b2, a[10 + cb], y[cb]);
      SBAR();
#pragma unroll
      for (int cb = 0; cb < 5; ++cb) { y[cb] = mfma(b3, a[15 + cb], y[cb]); SBAR(); }
#pragma unroll
      for (int cb = 0; cb < 5; ++cb) *(LAS f32x4*)((LAS char*)lds + yd + cb * 1280) = y[cb];
      wg_barrier();
      const LAS f32x4* p = (const LAS f32x4*)((LAS char*)lds + ys);
      const f32x4 t0 = p[0], t1 = p[400], t2 = p[800], t3 = p[1200], t4 = p[320], t5 = p[720], t6 = p[1120], t7 = p[1520];
      stsc(rw, po, pack4((t0 + t1) + (t2 + t3), tag));
      stsc(rw, po2, pack4((t4 + t5) + (t6 + t7), tag));
      STAMP(30);
    }
    STAMP_OUT();
    return;
  }
}

extern "C" void kernel_launch(void* const* d_in, const int* in_sizes, int n_in, void* d_out, int out_size, void* d_ws, size_t ws_size,
                              hipStream_t stream) {
  fused_decode<<<NWG, NTHR, 0, stream>>>((const u16*)d_in[1], (const u16*)d_in[2], (unsigned char*)d_ws, (const u16*)d_in[0], (const u16*)d_in[3],
                                          (const u16*)d_in[4], (const u16*)d_in[6], (const u16*)d_in[7], (const u16*)d_in[8], (u16*)d_out);
}
```

```cpp
#include <hip/hip_runtime.h>
#include <stdint.h>

typedef unsigned u32;
typedef unsigned short u16;
typedef unsigned long long u64;
typedef u32 u32x2 __attribute__((ext_vector_type(2)));
typedef u32 u32x3 __attribute__((ext_vector_type(3)));
typedef u32 u32x4 __attribute__((ext_vector_type(4)));
typedef u32 u32x16 __attribute__((ext_vector_type(16)));
typedef float f32x2 __attribute__((ext_vector_type(2)));
typedef float f32x4 __attribute__((ext_vector_type(4)));
typedef __bf16 bf16x8 __attribute__((ext_vector_type(8)));
typedef __bf16 bf16x2 __attribute__((ext_vector_type(2)));
typedef __amdgpu_buffer_rsrc_t rsrc_t;
#define GAS __attribute__((address_space(1)))
#define LAS __attribute__((address_space(3)))

constexpr int DM = 576, HQ = 32, SK = 32768, DH = 128, FF = 4608;
constexpr int NWG = 256, NTHR = 512;
constexpr u32 PAIR = SK * DH * 2;
constexpr int NU = 208, O0 = 64, F0 = 128, NF = 72, Z0 = 200;
constexpr u32 MA[18] = {0xa4, 0x180, 0x21e4, 0x6194, 0x7730, 0x15bc, 0x1a28, 0x21a4, 0x2024, 0x90,
                        0x100, 0x2120, 0x7294, 0x3030, 0x4080, 0x1d5c, 0x508, 0x185c};
constexpr u32 MC[8] = {0x23, 0x8, 0x94, 0x1, 0x10, 0x2, 0x40, 0x80};
template <int LO, int N> __host__ __device__ constexpr u32 mapa(u32 v) {
  u32 r = 0;
  for (int j = 0; j < N; ++j) r ^= ((v >> j) & 1) ? MA[LO + j] << 8 : 0u;
  return r;
}
constexpr int gf2_rank(const u32* cols, u32 use, int n) {
  u32 basis[32] = {};
  int rank = 0;
  for (int j = 0; j < n; ++j) {
    u32 v = ((use >> j) & 1) ? cols[j] : 0u;
    for (int b = 31; b >= 0 && v; --b)
      if ((v >> b) & 1) {
        if (!basis[b]) { basis[b] = v; ++rank; break; }
        v ^= basis[b];
      }
  }
  return rank;
}
constexpr u32 ma_all() { u32 r = 0; for (int j = 0; j < 18; ++j) r |= MA[j]; return r; }
static_assert(gf2_rank(MC, 0xff, 8) == 8, "every (pair, range) has one workgroup");
static_assert((ma_all() & ~0x7ffcu) == 0 && gf2_rank(MA, 0x3ff07, 18) == 13, "(range, wave, step) covers every 1 KB block of a pair once");

constexpr int NFL = 5, NHI = 9;
constexpr int NAUX = 9, NLATE = 8;
constexpr int QPOS = 26;
constexpr int IDLE = 58;
constexpr int DT0 = 205, DT_MIN = 203, DT_MAX = 235, LAG_SH = 3, BIAS_K = 4, BIAS_V = -8, LEAD_O = 99, LEAD_F = 76;
constexpr u32 SC_MASK = 0x4924;
constexpr u64 TAB_UP = 0x0011495557777FFFull, TAB_DN = 0x5555555549110100ull;
constexpr int CODE_BYTES = 18432;

constexpr int AUX_NT = 2, AUX_SC1 = 16;
constexpr u32 OOB = 0x80000000u;
constexpr u32 SPIN_CAP = 400000;
constexpr u32 WS_E = 0, WS_PG = 4096, WS_AG = WS_PG + NU * 1024, WS_YG = WS_AG + NWG * 1024, WS_FG = WS_YG + 36 * NF * 128,
              WS_SC = WS_FG + 36 * NF * 128, SC_STR = 4352, SC_ROW = 8 * SC_STR, WS_END = WS_SC + 16 * SC_ROW;
constexpr int L_CTR = 0, L_TAG = 4, L_ML = 8, L_ACC = 32, ACC_W = 352, L_XN = L_ACC + 4 * ACC_W, L_RED = L_XN + 4 * 576, L_Y = L_RED + 4 * ACC_W,
              L_S = L_Y + 4 * 1600, L_END = L_S + 4 * 2048;
constexpr float LOG2E = 1.4426950408889634f;

__device__ __forceinline__ rsrc_t mkrsrc(const void* p, u32 bytes) { return __builtin_amdgcn_make_buffer_rsrc((void*)p, 0, bytes, 0x00020000); }
__device__ __forceinline__ const char* mkptr(u32 lo, u32 hi) { return (const char*)(GAS char*)(((u64)hi << 32) | lo); }
__device__ __forceinline__ u32x4 ldnt(rsrc_t r, u32 vo, u32 so) { return __builtin_amdgcn_raw_buffer_load_b128(r, vo, so, AUX_NT); }
__device__ __forceinline__ u32x4 ldsc(rsrc_t r, u32 vo, u32 so) { return __builtin_amdgcn_raw_buffer_load_b128(r, vo, so, AUX_SC1); }
__device__ __forceinline__ void stsc(rsrc_t r, u32 vo, u32x4 v) { __builtin_amdgcn_raw_buffer_store_b128(v, r, vo, 0, AUX_SC1); }
__device__ __forceinline__ f32x4 mfma(u32x4 a, u32x4 b, f32x4 c) {
  return __builtin_amdgcn_mfma_f32_16x16x32_bf16(__builtin_bit_cast(bf16x8, a), __builtin_bit_cast(bf16x8, b), c, 0, 0, 0);
}
__device__ __forceinline__ float asf(u32 x) { return __builtin_bit_cast(float, x); }
__device__ __forceinline__ u32 asu(float x) { return __builtin_bit_cast(u32, x); }
__device__ __forceinline__ u32 pk(float lo, float hi) { bf16x2 v = {(__bf16)lo, (__bf16)hi}; return __builtin_bit_cast(u32, v); }
__device__ __forceinline__ float rbf(float x) { return (float)(__bf16)x; }
__device__ __forceinline__ float lo16(u32 x) { return asf(x << 16); }
__device__ __forceinline__ float hi16(u32 x) { return asf(x & 0xffff0000u); }
template <int CTRL> __device__ __forceinline__ u32 dppu(u32 x) { return (u32)__builtin_amdgcn_mov_dpp((int)x, CTRL, 0xf, 0xf, true); }
template <int CTRL> __device__ __forceinline__ float dppf(float x) { return asf(dppu<CTRL>(asu(x))); }
constexpr int XOR1 = 0xB1, XOR2 = 0x4E, XOR7 = 0x141, ROR4 = 0x124, ROR8 = 0x128;
__device__ __forceinline__ float sum16(float x) { auto s = __builtin_amdgcn_permlane16_swap(asu(x), asu(x), false, false); return asf(s[0]) + asf(s[1]); }
__device__ __forceinline__ float sum32(float x) { auto s = __builtin_amdgcn_permlane32_swap(asu(x), asu(x), false, false); return asf(s[0]) + asf(s[1]); }
__device__ __forceinline__ float max16(float x) { auto s = __builtin_amdgcn_permlane16_swap(asu(x), asu(x), false, false); return fmaxf(asf(s[0]), asf(s[1])); }
__device__ __forceinline__ float max32(float x) { auto s = __builtin_amdgcn_permlane32_swap(asu(x), asu(x), false, false); return fmaxf(asf(s[0]), asf(s[1])); }
__device__ __forceinline__ float ex2(float x) { return __builtin_amdgcn_exp2f(x); }
__device__ __forceinline__ float rcp(float x) { return __builtin_amdgcn_rcpf(x); }
__device__ __forceinline__ float silu(float x) { return x * rcp(1.0f + ex2(-LOG2E * x)); }
__device__ __forceinline__ u32 now32() { return (u32)__builtin_amdgcn_s_memrealtime(); }
#define SBAR() __builtin_amdgcn_sched_barrier(0)
__device__ __forceinline__ void lds_fence() { asm volatile("s_waitcnt lgkmcnt(0)" ::: "memory"); }
__device__ __forceinline__ void wg_barrier() { lds_fence(); __builtin_amdgcn_s_barrier(); asm volatile("" ::: "memory"); }
template <int N> __device__ __forceinline__ void pace() { asm volatile("s_waitcnt vmcnt(%0)" ::"n"(N - 1) : "memory"); }

__device__ __forceinline__ u32 next_tag(u32 e) { return (e * 1664525u + 1013904223u) | 0x80000000u; }
__device__ __forceinline__ u32x4 pack4(f32x4 v, u32 tag) {
  return u32x4{__builtin_amdgcn_perm(asu(v[1]), asu(v[0]), 0x05030201u), __builtin_amdgcn_perm(tag, asu(v[1]), 0x05040302u),
               __builtin_amdgcn_perm(asu(v[3]), asu(v[2]), 0x05030201u), __builtin_amdgcn_perm(tag, asu(v[3]), 0x05040302u)};
}
__device__ __forceinline__ f32x4 unpack4(u32x4 r) {
  return f32x4{asf(r[0] << 8), asf(__builtin_amdgcn_perm(r[1], r[0], 0x0504030cu)), asf(r[2] << 8), asf(__builtin_amdgcn_perm(r[3], r[2], 0x0504030cu))};
}
template <int N> __device__ __forceinline__ bool tags_ok(const u32x4* r, u32 tag) {
  u32 bad = 0;
#pragma unroll
  for (int i = 0; i < N; ++i) bad |= (r[i][1] ^ (tag << 16)) | (r[i][3] ^ (tag << 16));
  return __builtin_amdgcn_ballot_w64(bad > 0xffffu) == 0ull;
}
#define G_LD(M, R, SO, OFF) "s_wqm_b64 exec, " M "\n\tbuffer_load_dwordx4 " R ", %[vo], %[rs], " SO " offen " OFF " sc1\n\t"
#define G_CK(M, A, B) "v_bitop3_b32 %[t], " A ", " B ", %[tg] bitop3:0x7e\n\tv_cmp_lt_u32_e64 " M ", %[lim], %[t]\n\t"
#define G_END "s_cbranch_scc0 2f\n\ts_sub_u32 %[n], %[n], 1\n\ts_cbranch_scc0 1b\n2:"
__device__ __forceinline__ u32x4 gather1(u32x4 rs, u32 vo, u32 t16) {
  u32x4 r; u64 m0; u32 t, n = SPIN_CAP;
  asm volatile("1:\n\tbuffer_load_dwordx4 v[140:143], %[vo], %[rs], 0 offen sc1\n\ts_waitcnt vmcnt(0)\n\t" G_CK("%[m0]", "v141", "v143")
               "s_cmp_lg_u64 %[m0], 0\n\t" G_END
               : "=&{v[140:143]}"(r), [m0] "=&s"(m0), [t] "=&v"(t), [n] "+s"(n) : [vo] "v"(vo), [rs] "s"(rs), [tg] "s"(t16), [lim] "v"(0xffffu) : "memory", "scc");
  return r;
}
template <int N> __device__ __forceinline__ void gather(u32x4 (&r)[5], u32x4 rs, u32 vo, u32 t16) {
  u64 m0 = ~0ull, m1 = ~0ull, m2 = ~0ull, m3 = ~0ull, m4 = N > 4 ? 0xffffffffull : 0ull, any; u32 t, n = SPIN_CAP;
  r[4] = u32x4{0u, t16, 0u, t16};
  asm volatile("1:\n\t" G_LD("%[m0]", "v[40:43]", "0", "") G_LD("%[m1]", "v[44:47]", "0", "offset:2048") G_LD("%[m2]", "v[48:51]", "%[s4]", "")
               G_LD("%[m3]", "v[52:55]", "%[s4]", "offset:2048") ".if %[five]\n\t" G_LD("%[m4]", "v[56:59]", "%[s8]", "") ".endif\n\t"
               "s_mov_b64 exec, -1\n\ts_waitcnt vmcnt(0)\n\t"
               G_CK("%[m0]", "v41", "v43") G_CK("%[m1]", "v45", "v47") G_CK("%[m2]", "v49", "v51") G_CK("%[m3]", "v53", "v55")
               ".if %[five]\n\t" G_CK("%[m4]", "v57", "v59") ".endif\n\t"
               "s_or_b64 %[any], %[m0], %[m1]\n\ts_or_b64 %[any], %[any], %[m2]\n\ts_or_b64 %[any], %[any], %[m3]\n\t"
               ".if %[five]\n\ts_or_b64 %[any], %[any], %[m4]\n\t.endif\n\t" G_END
               : "=&{v[40:43]}"(r[0]), "=&{v[44:47]}"(r[1]), "=&{v[48:51]}"(r[2]), "=&{v[52:55]}"(r[3]), "+{v[56:59]}"(r[4]), [m0] "+s"(m0), [m1] "+s"(m1),
                 [m2] "+s"(m2), [m3] "+s"(m3), [m4] "+s"(m4), [any] "=&s"(any), [t] "=&v"(t), [n] "+s"(n)
               : [vo] "v"(vo), [rs] "s"(rs), [tg] "s"(t16), [lim] "v"(0xffffu), [s4] "s"(4096u), [s8] "s"(8192u), [five] "n"(N > 4) : "memory", "scc");
}
template <int N> __device__ __forceinline__ float sum_parts(const u32x4* r) {
  f32x4 a = unpack4(r[0]);
#pragma unroll
  for (int i = 1; i < N; ++i) a += unpack4(r[i]);
  const auto s0 = __builtin_amdgcn_permlane32_swap(asu(a[0]), asu(a[2]), false, false), s1 = __builtin_amdgcn_permlane32_swap(asu(a[1]), asu(a[3]), false, false);
  const auto s2 = __builtin_amdgcn_permlane16_swap(asu(asf(s0[0]) + asf(s0[1])), asu(asf(s1[0]) + asf(s1[1])), false, false);
  float t = asf(s2[0]) + asf(s2[1]);
  t += dppf<ROR4>(t); t += dppf<ROR8>(t);
  return t;
}

template <int BIT, int EXTRA> __device__ __forceinline__ void pacev(u32 mask) {
  asm volatile("s_bitcmp1_b32 %0, %4+8\n\ts_cbranch_scc0 1f\n\ts_waitcnt vmcnt(%3)\n1:\n\t"
               "s_bitcmp1_b32 %0, %4\n\ts_waitcnt vmcnt(%1)\n\ts_cbranch_scc0 2f\n\ts_waitcnt vmcnt(%2)\n2:"
               ::"s"(mask), "n"(NHI - 1 + EXTRA), "n"(NFL - 1 + EXTRA), "n"(NFL - 2 + EXTRA), "n"(BIT) : "memory", "scc");
}
__device__ __forceinline__ u32 pace_mask(u32 now, u32 due) {
  int up = (int)(now - due) >> LAG_SH, dn = -up;
  asm("s_max_i32 %0, %0, 0\n\ts_min_i32 %0, %0, 7\n\ts_max_i32 %1, %1, 0\n\ts_min_i32 %1, %1, 7" : "+s"(up), "+s"(dn) : : "scc");
  return ((u32)(TAB_UP >> (8 * up)) & 0xffu) | ((u32)(TAB_DN >> (8 * dn)) << 8);
}
struct Sched { u32 mask, due, on, tag, lead, ldo, sto, prev, dt; bool sample; u32x2 row; };
template <int GRP> __device__ __forceinline__ void sched_step(Sched& sc, u32 now, rsrc_t rw, int it) {
  if (GRP == 0) {
    sc.row = __builtin_amdgcn_raw_buffer_load_b64(rw, ((SC_MASK >> it) & 1) ? sc.ldo : OOB, it * SC_ROW, AUX_SC1);
    if (sc.sample && ((SC_MASK >> (it + 1)) & 1)) __builtin_amdgcn_raw_buffer_store_b64(u32x2{now + sc.lead, sc.tag}, rw, sc.sto, (it + 1) * SC_ROW, AUX_SC1);
  } else {
    const bool ok = __builtin_amdgcn_ballot_w64(sc.row[1] == sc.tag) == 0xffull;
    u32 d = sc.row[0] - now;
    d += dppu<XOR1>(d); d += dppu<XOR2>(d); d += dppu<XOR7>(d);
    const u32 mean = now + ((int)__builtin_amdgcn_readfirstlane(d) >> 3) - sc.lead;
    if (ok) {
      if (sc.on == (u32)it - 3u) {
        int m = (((int)((mean - sc.prev) * 683u) >> 12) + (int)sc.dt) >> 1;
        asm("s_max_i32 %0, %0, %1\n\ts_min_i32 %0, %0, %2" : "+s"(m) : "n"(DT_MIN), "n"(DT_MAX) : "scc");
        sc.dt = m;
      }
      sc.prev = mean; sc.on = it;
      sc.due = mean + 3 * sc.dt - (it >= 8 ? BIAS_V : BIAS_K);
    }
  }
  sc.mask = sc.on ? pace_mask(now, sc.due) : 0xffu;
  sc.due += sc.dt;
}
template <int GRP, int I>
__device__ __forceinline__ void refill(u32x4& slot, rsrc_t r, u32 vo, u32 so, Sched& sc, u32& now, rsrc_t rw, int it) {
  pacev<I, (GRP == 0 && I >= 2 && I < 2 + NFL) ? 1 : 0>(sc.mask);
  constexpr u32 step = mapa<10, 5>(GRP * 8 + I);
  slot = ldnt(r, vo, so ^ step);
  if (!(GRP & 1) && I == 0) now = now32();
  if (!(GRP & 1) && I == 1) sched_step<GRP>(sc, now, rw, it);
  if (GRP == 3 && I == 2 && it == 14 && sc.lead == 0) sc.mask = 0u;
  SBAR();
}

template <int W, int T> __device__ __forceinline__ u32 bank_mov(u32 old, u32 z) {
  constexpr int ctrl = W == T ? 0xE4 : (W > T ? 0x110 + 4 * (W - T) : 0x100 + 4 * (T - W));
  return (u32)__builtin_amdgcn_update_dpp((int)old, (int)z, ctrl, 0xf, 1 << W, false);
}
template <int T> __device__ __forceinline__ void k_pick(u32x4& u, f32x4 d) {
  u[0] = bank_mov<T, 0>(u[0], asu(d[0])); u[1] = bank_mov<T, 1>(u[1], asu(d[1])); u[2] = bank_mov<T, 2>(u[2], asu(d[2])); u[3] = bank_mov<T, 3>(u[3], asu(d[3]));
}
template <int GRP>
__device__ __forceinline__ float k_tile(u32x4 (&k)[8], u32x4 bq0, u32x4 bq1, u32x4& u, rsrc_t r, u32 vo, u32 so, Sched& sc, rsrc_t rw, int it) {
  const f32x4 z = {0.f, 0.f, 0.f, 0.f};
  f32x4 da, db;
  u32 now = 0;
#define RF(I) SBAR(); refill<GRP, I>(k[I], r, vo, so, sc, now, rw, it);
  da = mfma(k[0], bq0, z);                     RF(0)
  da = mfma(k[1], bq1, da);                    RF(1)
  db = mfma(k[2], bq0, z);                     RF(2)
  db = mfma(k[3], bq1, db); k_pick<0>(u, da);  RF(3)
  da = mfma(k[4], bq0, z);                     RF(4)
  da = mfma(k[5], bq1, da); k_pick<1>(u, db);  RF(5)
  db = mfma(k[6], bq0, z);                     RF(6)
  db = mfma(k[7], bq1, db); k_pick<2>(u, da);  RF(7)
#undef RF
  k_pick<3>(u, db);
  const float s = (asf(u[0]) + asf(u[1])) + (asf(u[2]) + asf(u[3]));
  return rbf(rbf(s) * 0.08838834764831845f);
}
__device__ __forceinline__ u32x2 probs(float s, float ml, float& l, u32 hm) {
  const float p = ex2(fmaf(s, LOG2E, -ml));
  l += p;
  const u32 t = pk(p, p);
  return u32x2{dppu<0xA0>(t) & hm, dppu<0xF5>(t) & hm};
}
template <int T> __device__ __forceinline__ void build_p(u32x4& b, u32 z) {
  b[0] = bank_mov<0, T>(b[0], z); b[1] = bank_mov<1, T>(b[1], z); b[2] = bank_mov<2, T>(b[2], z); b[3] = bank_mov<3, T>(b[3], z);
}
template <int GRP>
__device__ __forceinline__ void v_tile(u32x4 (&v)[8], u32x2 z, f32x4& acc, u32x4& bp0, u32x4& bp1, rsrc_t r, u32 vo, u32 so, Sched& sc, rsrc_t rw, int it) {
  u32 now = 0;
#define PV_STEP(U, BP) build_p<(U) / 2>(BP, z[(U) & 1]); acc = mfma(v[U], BP, acc); SBAR(); refill<GRP, U>(v[U], r, vo, so, sc, now, rw, it);
  PV_STEP(0, bp0) PV_STEP(1, bp1) PV_STEP(2, bp0) PV_STEP(3, bp1) PV_STEP(4, bp0) PV_STEP(5, bp1) PV_STEP(6, bp0) PV_STEP(7, bp1)
#undef PV_STEP
}
template <int K> __device__ __forceinline__ u32x4 row_load(rsrc_t r, u32 vo, u32 vo4) { return ldnt(r, K % 5 == 4 ? vo4 : vo, (K / 5) * 1152 + (K % 5) * 256); }
template <int LO, int HI> __device__ __forceinline__ void rows_load(u32x4 (&a)[20], rsrc_t r, u32 vo, u32 vo4) {
  if constexpr (LO < HI) { pace<NLATE>(); a[LO] = row_load<LO>(r, vo, vo4); SBAR(); rows_load<LO + 1, HI>(a, r, vo, vo4); }
}
template <int FROM, int CNT, int K0>
__device__ __forceinline__ void v_last(const u32x4 (&v)[8], u32x2 z, f32x4& acc, u32x4& bp0, u32x4& bp1, u32x4 (&a)[20], rsrc_t r, u32 vo, u32 vo4) {
#define PV_STEP(U, BP) build_p<(U) / 2>(BP, z[(U) & 1]); acc = mfma(v[U], BP, acc); SBAR(); \
  if constexpr (U >= FROM && U < FROM + CNT) { a[K0 + U - FROM] = row_load<K0 + U - FROM>(r, vo, vo4); SBAR(); }
  PV_STEP(0, bp0) PV_STEP(1, bp1) PV_STEP(2, bp0) PV_STEP(3, bp1) PV_STEP(4, bp0) PV_STEP(5, bp1) PV_STEP(6, bp0) PV_STEP(7, bp1)
#undef PV_STEP
}

struct Sel { u32 s[4][2]; };
__device__ __forceinline__ Sel make_sel(int lane) {
  Sel r; const int wp = (lane >> 2) & 3; const bool hb = lane & 1;
#pragma unroll
  for (int par = 0; par < 2; ++par) {
    const u32 two = ((2 * par + 1) << 8) | (2 * par);
    const u32 base = hb ? ((two << 16) | 0x0c0cu) : (0x0c0c0000u | two);
#pragma unroll
    for (int w = 0; w < 4; ++w) r.s[w][par] = wp == w ? base : 0x0c0c0c0cu;
  }
  return r;
}
__device__ __forceinline__ u32x4 build_b(u32 d, const Sel& sel, int par) {
  u32x4 b;
#pragma unroll
  for (int w = 0; w < 4; ++w) b[w] = __builtin_amdgcn_perm(d, d, sel.s[w][par]);
  return b;
}
template <int LO, int HI, int N> __device__ __forceinline__ void unit_load(u32x4 (&a)[18], rsrc_t r, u32 vo, u32 stride) {
  if constexpr (LO < HI) {
    if constexpr (N > 0) pace<N>();
    a[LO] = ldnt(r, vo, (32 * (LO >> 1) + (LO & 1)) * stride);
    SBAR();
    unit_load<LO + 1, HI, N>(a, r, vo, stride);
  }
}
__device__ __forceinline__ void layernorm(const u32x3 (&xr)[3], LAS u32* xn, int lane) {
  f32x2 v[9], s2 = {0.f, 0.f};
#pragma unroll
  for (int k = 0; k < 9; ++k) { v[k] = f32x2{lo16(xr[k / 3][k % 3]), hi16(xr[k / 3][k % 3])}; s2 += v[k]; }
  float s = s2[0] + s2[1];
  s += dppf<XOR1>(s); s += dppf<XOR2>(s); s += dppf<ROR8>(s); s += dppf<ROR4>(s); s = sum16(s);
  const float mean = s * (1.0f / DM);
  f32x2 q2 = {0.f, 0.f};
#pragma unroll
  for (int k = 0; k < 9; ++k) { const f32x2 a = v[k] - mean; q2 += a * a; }
  float q = q2[0] + q2[1];
  q += dppf<XOR1>(q); q += dppf<XOR2>(q); q += dppf<ROR8>(q); q += dppf<ROR4>(q); q = sum16(q);
  const float mb = rbf(mean), den = rbf(__builtin_amdgcn_sqrtf(rbf(rbf(q * (1.0f / DM)) + 1e-5f))), inv = rcp(den);
#pragma unroll
  for (int k = 0; k < 9; ++k) {
    const f32x2 a = v[k] - mb;
    const u32 r = pk(a[0], a[1]);
    const f32x2 b = f32x2{lo16(r), hi16(r)} * inv;
    xn[(lane >> 5) * 288 + (lane & 31) * 3 + 96 * (k / 3) + k % 3] = pk(b[0], b[1]);
  }
  lds_fence();
}

__global__ void __launch_bounds__(NTHR, 2) __attribute__((aligned(4096)))
fused_decode(const u16*, const u16*, unsigned char*, const u16*, const u16*, const u16*, const u16*, const u16*, const u16*, u16*) {
  __shared__ __attribute__((aligned(16))) u32 smem[L_END];
  LAS u32* lds = (LAS u32*)smem;
  u32x16 ar; u32x4 ar2;
  asm volatile("s_load_dwordx16 %0, %2, 0x0\n\ts_load_dwordx4 %1, %2, 0x40" : "=&s"(ar), "=&s"(ar2) : "s"(__builtin_amdgcn_kernarg_segment_ptr()));
  SBAR();
  const int lane = threadIdx.x & 63, wave = __builtin_amdgcn_readfirstlane(threadIdx.x >> 6), bid = blockIdx.x;
  {
    u64 pc;
    asm volatile("s_cmp_lt_u32 %1, 3\n\ts_cbranch_scc0 1f\n\t"
                 "s_getpc_b64 %0\n\tv_mbcnt_lo_u32_b32 v1, -1, 0\n\tv_mbcnt_hi_u32_b32 v1, -1, v1\n\tv_lshl_add_u32 v1, %1, 6, v1\n\t"
                 "v_lshlrev_b32 v1, 7, v1\n\tv_min_u32 v1, %2, v1\n\tglobal_load_dword v1, v1, %0\n\ts_waitcnt vmcnt(0)\n1:"
                 : "=&s"(pc) : "s"((u32)(wave - 4) | ((u32)((bid >> 4) - (NU >> 4)) << 4)), "n"(CODE_BYTES) : "scc", "v1");
  }
  const int g = lane >> 4, c = lane & 15, aw = wave - 4;
  const u32 stride = bid < 64 ? HQ * DH * 2 : FF * 2;
  u32 uo = (288 * (bid & 1) + 8 * aw + 2 * g) * stride + ((bid >> 1) - (bid < 64 ? 0 : (bid < 136 ? 32 : 68))) * 256 + c * 16;
  asm volatile("" : "+v"(uo));
  SBAR();
  asm volatile("s_waitcnt lgkmcnt(0)" : "+s"(ar), "+s"(ar2));
  const char *kc = mkptr(ar[0], ar[1]), *vc = mkptr(ar[2], ar[3]), *x = mkptr(ar[6], ar[7]), *wq = mkptr(ar[8], ar[9]), *wo = mkptr(ar[10], ar[11]),
             *wg = mkptr(ar[12], ar[13]), *wu = mkptr(ar[14], ar[15]), *wd = mkptr(ar2[0], ar2[1]);
  char *ws = (char*)mkptr(ar[4], ar[5]), *out = (char*)mkptr(ar2[2], ar2[3]);
  const rsrc_t rw = mkrsrc(ws, WS_END);
  const u32x4 rws = {ar[4], ar[5] & 0xffffu, WS_END, 0x00020000u};

  if (__builtin_expect(wave >= 4, 1)) {
    if (__builtin_expect(bid < NU, 1)) {
      const rsrc_t ru = mkrsrc(bid < 64 ? wq : (bid < 136 ? wg : wu), DM * stride), rx = mkrsrc(x, 2 * DM * 2);
      u32x4 a[18];
      u32x3 xr[3];
      unit_load<0, NAUX, 0>(a, ru, uo, stride);
#pragma unroll
      for (int k = 0; k < 3; ++k) xr[k] = __builtin_amdgcn_raw_buffer_load_b96(rx, ((lane >> 5) * 288 + (lane & 31) * 3) * 4, k * 384, 0);
      const u32 e = __builtin_amdgcn_raw_buffer_load_b32(rw, WS_E, 0, AUX_SC1);
      SBAR();
      unit_load<NAUX, 18, NAUX + 4>(a, ru, uo, stride);
      wg_barrier();
      SBAR();
      const Sel sel = make_sel(lane);
      LAS u32* xn = lds + L_XN + aw * 576;
      layernorm(xr, xn, lane);
      u32 xm[9];
#pragma unroll
      for (int k = 0; k < 9; ++k) xm[k] = xn[((lane >> 1) & 1) * 288 + 144 * (bid & 1) + 4 * aw + g + 16 * k];
      f32x4 d = {0.f, 0.f, 0.f, 0.f};
#pragma unroll
      for (int k = 0; k < 18; ++k) d = mfma(a[k], build_b(xm[k >> 1], sel, k & 1), d);
      LAS float* red = (LAS float*)lds + L_RED;
#pragma unroll
      for (int i = 0; i < 4; ++i) red[aw * ACC_W + ((lane >> 1) & 1) * 176 + g * 40 + 8 * i + ((lane >> 2) & 3) * 2 + (lane & 1)] = d[i];
      lds_fence();
      u32 ticket = 0;
      if (lane == 0) ticket = __hip_atomic_fetch_add(lds + L_CTR, 1u, __ATOMIC_RELAXED, __HIP_MEMORY_SCOPE_WORKGROUP);
      if (__builtin_amdgcn_readfirstlane(ticket) == 3u) {
        asm volatile("" ::: "memory");
        const LAS float* p = red + (lane >> 5) * 176 + ((lane >> 3) & 3) * 40 + (lane & 7) * 4;
        const f32x4 s = (*(const LAS f32x4*)p + *(const LAS f32x4*)(p + ACC_W)) + (*(const LAS f32x4*)(p + 2 * ACC_W) + *(const LAS f32x4*)(p + 3 * ACC_W));
        stsc(rw, WS_PG + bid * 1024 + lane * 16, pack4(__builtin_bit_cast(f32x4, __builtin_bit_cast(u32x4, s) + 0x80u), next_tag(__builtin_amdgcn_readfirstlane(e))));
      }
    }
    if ((bid < O0 || (bid >= Z0 && bid < Z0 + 8)) && aw < 2) {
      const int f = bid < O0 ? bid : bid - (Z0 - O0);
      const u32 oi = (lane >> 5) * DM + 8 * f + 2 * (lane & 3) + (g & 1);
      u32 xv = ((const GAS u16*)x)[oi];
      u32 op = (lane & 12) == 0 ? oi * 2 : OOB;
      u32 go = (aw ? WS_FG : WS_YG) + (f >> 1) * (NF * 128) + (lane >> 2) * 128 + (f & 1) * 64 + (lane & 3) * 16;
      asm volatile("" : "+v"(xv), "+v"(op), "+v"(go));
      wg_barrier();
      const u32 tag = lds[L_TAG];
      LAS float* lg = (LAS float*)lds + L_RED + lane;
      const u32 t16 = __builtin_amdgcn_readfirstlane(tag) << 16;
      u32x4 r[5];
      if (aw) {
        gather<5>(r, rws, go, t16);
        *lg = rbf(silu(rbf(sum_parts<5>(r))));
        wg_barrier();
        return;
      }
      gather<4>(r, rws, go, t16);
      wg_barrier();
      float gt = *lg;
      SBAR();
      float sm = rbf(sum_parts<4>(r));
      asm volatile("" : "+v"(gt), "+v"(sm));
      __builtin_amdgcn_raw_buffer_store_b16((u16)pk(lo16(xv) + rbf(sm * gt), 0.f), mkrsrc(out, 2 * DM * 2), op, 0, 0);
      if (f == 0 && lane == 0) __builtin_amdgcn_raw_buffer_store_b32(tag, rw, WS_E, 0, AUX_SC1);
      return;
    }
    if (aw == 3 && (bid >> 3) == 28) {
      const u32 t0 = __builtin_amdgcn_raw_buffer_load_b32(mkrsrc(x, 4), lane * 4, 0, 0), t1 = __builtin_amdgcn_raw_buffer_load_b32(mkrsrc(ws, 4), lane * 4, 0, AUX_SC1);
      asm volatile("" ::"v"(t0), "v"(t1));
    }
    return;
  }

  u32 cid = 0;
#pragma unroll
  for (int j = 0; j < 8; ++j) cid ^= ((bid >> j) & 1) ? MC[j] : 0u;
  u32 s0 = mapa<0, 10>(cid | (wave << 8));
  u32 ko = ((lane >> 2) & 3) * 256 + (4 * g + (lane & 3)) * 16;
  u32 qo = WS_PG + ((cid >> 3) & 15) * 4096 + ((lane >> 1) & 1) * 2048 + (cid >> 7) * 512 + (lane >> 2) * 32 + (lane & 1) * 16;
  asm volatile("" : "+s"(cid), "+s"(s0));
  asm volatile("" : "+v"(ko), "+v"(qo));
  const rsrc_t rk = mkrsrc(kc + (size_t)(cid >> 3) * PAIR, PAIR), rv = mkrsrc(vc + (size_t)(cid >> 3) * PAIR, PAIR);
  const bool odd = lane & 1;
  const u32 vo = lane * 16;
  if (bid < NU) { lds[L_CTR] = 0u; wg_barrier(); } else __builtin_amdgcn_s_sleep(IDLE);
  SBAR();
  u32x4 r0[8], r1[8], r2[8], r3[8], qg[2];
  u32 e = 0;
#pragma unroll
  for (int p = 0; p < 32; ++p) {
    if (p == QPOS) {
      e = __builtin_amdgcn_raw_buffer_load_b32(rw, WS_E, 0, AUX_SC1);
      qg[0] = ldsc(rw, qo, 0); qg[1] = ldsc(rw, qo, 1024);
    }
    if (p >= QPOS && p < QPOS + NFL) pace<NFL + 3>();
    else if (p >= NFL) pace<NFL>();
    const u32x4 t = ldnt(rk, ko, s0 ^ mapa<10, 5>(p));
    if (p < 8) r0[p] = t; else if (p < 16) r1[p - 8] = t; else if (p < 24) r2[p - 16] = t; else r3[p - 24] = t;
    SBAR();
  }
  const u32 tag = next_tag(__builtin_amdgcn_readfirstlane(e));
  if (!tags_ok<2>(qg, tag)) {
    u32 spins = SPIN_CAP;
    _Pragma("nounroll") do { asm volatile("" ::: "memory"); qg[0] = ldsc(rw, qo, 0); qg[1] = ldsc(rw, qo, 1024); } while (!tags_ok<2>(qg, tag) && --spins);
  }
  const bool isO = bid >= O0 && bid < F0, isF = bid >= F0 && bid < Z0;
  Sched sc;
  sc.mask = 0xffu; sc.due = 0u; sc.on = 0u; sc.tag = tag; sc.prev = 0u; sc.dt = DT0;
  sc.lead = isO ? LEAD_O : (isF ? LEAD_F : 0);
  sc.ldo = lane < 8 ? WS_SC + lane * SC_STR : OOB;
  sc.sample = bid == 33 * (bid & 7) && wave == (bid & 3);
  sc.sto = lane == 0 ? WS_SC + (bid & 7) * SC_STR : OOB;

  LAS f32x4* sl = (LAS f32x4*)(lds + L_S) + wave * 512 + lane;
  float ml;
  {
    const f32x4 qf = unpack4(qg[0]) + unpack4(qg[1]);
    const u32 q0 = pk(qf[0], qf[1]), q1 = pk(qf[2], qf[3]), p0 = dppu<XOR1>(q0), p1 = dppu<XOR1>(q1);
    const u32x4 bq0 = {odd ? 0u : q0, odd ? 0u : q1, odd ? 0u : p0, odd ? 0u : p1}, bq1 = {odd ? p0 : 0u, odd ? p1 : 0u, odd ? q0 : 0u, odd ? q1 : 0u};
    u32x4 u = {0u, 0u, 0u, 0u};
    float mx = -INFINITY;
    for (int it = 0; it < 8; ++it) {
      int itv = it; asm volatile("" : "+s"(itv));
      const bool last = itv == 7;
      const rsrc_t r = mkrsrc((last ? vc : kc) + (size_t)(cid >> 3) * PAIR, PAIR);
      const u32 lo = last ? vo : ko, so = s0 ^ mapa<15, 3>(it + 1);
      f32x4 s4;
      s4[0] = k_tile<0>(r0, bq0, bq1, u, r, lo, so, sc, rw, it);
      s4[1] = k_tile<1>(r1, bq0, bq1, u, r, lo, so, sc, rw, it);
      s4[2] = k_tile<2>(r2, bq0, bq1, u, r, lo, so, sc, rw, it);
      s4[3] = k_tile<3>(r3, bq0, bq1, u, r, lo, so, sc, rw, it);
      sl[it * 64] = s4;
      mx = fmaxf(fmaxf(mx, fmaxf(s4[0], s4[1])), fmaxf(s4[2], s4[3]));
    }
    mx = fmaxf(mx, dppf<XOR1>(mx)); mx = fmaxf(mx, dppf<ROR4>(mx)); mx = fmaxf(mx, dppf<ROR8>(mx));
    ml = max32(max16(mx)) * LOG2E;
  }
  const int u = bid - (isF ? F0 : O0);
  const u32 ho = WS_PG + (64 + 72 * (lane & 1) + (u & ~1) + ((lane >> 2) & 1)) * 1024 + ((lane >> 1) & 1) * 512 + (u & 1) * 256 + (4 * wave + g) * 16;
  u32x4 hq = ldsc(rw, isF ? ho : OOB, 0);
  float l = 0.f;
  f32x4 acc = {0.f, 0.f, 0.f, 0.f};
  u32x4 bp0 = {0u, 0u, 0u, 0u}, bp1 = {0u, 0u, 0u, 0u};
  const u32 hm = odd ? 0xffff0000u : 0x0000ffffu;
  f32x4 s4 = sl[0];
  for (int it2 = 8; it2 < 15; ++it2) {
    int it = it2; asm volatile("" : "+s"(it));
    const u32 so = s0 ^ mapa<15, 3>(it + 1);
    const f32x4 sn = sl[(it - 7) * 64];
    v_tile<0>(r0, probs(s4[0], ml, l, hm), acc, bp0, bp1, rv, vo, so, sc, rw, it);
    v_tile<1>(r1, probs(s4[1], ml, l, hm), acc, bp0, bp1, rv, vo, so, sc, rw, it);
    v_tile<2>(r2, probs(s4[2], ml, l, hm), acc, bp0, bp1, rv, vo, so, sc, rw, it);
    v_tile<3>(r3, probs(s4[3], ml, l, hm), acc, bp0, bp1, rv, vo, so, sc, rw, it);
    s4 = sn;
  }
  const rsrc_t rl = mkrsrc(isF ? wd : wo, isF ? FF * DM * 2 : (isO ? HQ * DH * DM * 2 : 0));
  const u32 wv = (64 * u + 16 * wave + 4 * g) * 1152 + c * 16, wv4 = c < 8 ? wv : OOB;
  u32x4 a[20];
  v_last<0, 1, 0>(r0, probs(s4[0], ml, l, hm), acc, bp0, bp1, a, rl, wv, wv4);
  v_last<0, 0, 0>(r1, probs(s4[1], ml, l, hm), acc, bp0, bp1, a, rl, wv, wv4);
  v_last<0, 0, 0>(r2, probs(s4[2], ml, l, hm), acc, bp0, bp1, a, rl, wv, wv4);
  const u32x2 z3 = probs(s4[3], ml, l, hm);
  l += dppf<XOR1>(l); l += dppf<ROR4>(l); l += dppf<ROR8>(l); l = sum32(sum16(l));
  LAS float* lf = (LAS float*)lds;
  if (lane < 4) lf[L_ML + lane * 4 + wave] = odd ? l : ml;
  lds[L_TAG] = tag;
  u32 ad = (L_ACC + wave * ACC_W + ((lane >> 1) & 1) * 176 + g * 40 + ((lane >> 2) & 3) * 2 + (lane & 1)) * 4;
  u32 as = (L_ACC + (lane >> 5) * 176 + ((lane >> 3) & 3) * 40 + (lane & 7) * 4) * 4, am = (L_ML + (lane >> 5) * 8) * 4;
  u32 ao = WS_AG + (((cid >> 3) & 15) * 16 + (lane >> 2)) * 1024 + (((cid >> 1) & 3) * 4 + (cid >> 7) * 2 + (cid & 1)) * 64 + (lane & 3) * 16;
  asm volatile("" : "+v"(ao), "+v"(ad), "+v"(as), "+v"(am));
  SBAR();
  v_last<3, 5, 1>(r3, z3, acc, bp0, bp1, a, rl, wv, wv4);
#pragma unroll
  for (int i = 0; i < 4; ++i) *(LAS float*)((LAS char*)lds + ad + 32 * i) = acc[i];
  wg_barrier();
  if (wave == 0) {
    const f32x4 mw = *(const LAS f32x4*)((LAS char*)lds + am), lw = *(const LAS f32x4*)((LAS char*)lds + am + 16);
    f32x4 pa[4];
#pragma unroll
    for (int w = 0; w < 4; ++w) pa[w] = *(const LAS f32x4*)((LAS char*)lds + as + w * (ACC_W * 4));
    SBAR();
    const float M = fmaxf(fmaxf(mw[0], mw[1]), fmaxf(mw[2], mw[3]));
    const f32x4 ew = {ex2(mw[0] - M), ex2(mw[1] - M), ex2(mw[2] - M), ex2(mw[3] - M)};
    const float L = fmaf(ew[0], lw[0], ew[1] * lw[1]) + fmaf(ew[2], lw[2], ew[3] * lw[3]);
    const f32x4 o = (ew[0] * pa[0] + ew[1] * pa[1] + ew[2] * pa[2] + ew[3] * pa[3]) * rcp(L);
    const u32 lse = asu(M + __builtin_amdgcn_logf(L));
    stsc(rw, ao, u32x4{pk(o[0], o[1]), __builtin_amdgcn_perm(tag, lse, 0x05040100u), pk(o[2], o[3]), __builtin_amdgcn_perm(tag, lse, 0x05040302u)});
  }
  if (!isO && !isF) return;

  rows_load<6, 20>(a, rl, wv, wv4);
  Sel sel = make_sel(lane);
  u32 po = (isF ? WS_FG : WS_YG) + ((wave * 8 + (lane >> 3)) * NF + u) * 128 + (lane & 7) * 16, po2 = (wave == 0 && lane < 32) ? po + 32 * NF * 128 : OOB;
  u32 yd = L_Y * 4 + (wave * 400 + g * 20 + c) * 16, ys = L_Y * 4 + (wave * 80 + (lane & 3) * 20 + (lane >> 2)) * 16;
  asm volatile("" : "+v"(sel.s[0][0]), "+v"(sel.s[0][1]), "+v"(sel.s[1][0]), "+v"(sel.s[1][1]), "+v"(sel.s[2][0]), "+v"(sel.s[2][1]), "+v"(sel.s[3][0]),
               "+v"(sel.s[3][1]), "+v"(po), "+v"(po2), "+v"(yd), "+v"(ys));
  u32 m0, m1;
  if (isO) {
    const u32x4 r = gather1(rws, WS_AG + (u * 4 + wave) * 1024 + c * 64 + g * 16, tag << 16);
    const float lse = asf(__builtin_amdgcn_perm(r[3], r[1], 0x05040100u));
    float M = fmaxf(lse, dppf<XOR1>(lse)); M = fmaxf(M, dppf<ROR4>(M)); M = fmaxf(M, dppf<ROR8>(M));
    const float wt = ex2(lse - M);
    float o[5] = {wt * lo16(r[0]), wt * hi16(r[0]), wt * lo16(r[2]), wt * hi16(r[2]), wt};
#define RED5(C) _Pragma("unroll") for (int i = 0; i < 5; ++i) { o[i] += dppf<C>(o[i]); asm volatile("" : "+v"(o[i])); }
    asm volatile("" : "+v"(o[0]), "+v"(o[1]), "+v"(o[2]), "+v"(o[3]));
    RED5(XOR1) RED5(ROR8) RED5(ROR4)
#undef RED5
    const float inv = rcp(o[4]);
    m0 = pk(o[0] * inv, o[1] * inv); m1 = pk(o[2] * inv, o[3] * inv);
  } else {
    if (!tags_ok<1>(&hq, tag)) hq = gather1(rws, ho, tag << 16);
    f32x4 h = unpack4(hq);
#pragma unroll
    for (int i = 0; i < 4; ++i) {
      const float own = rbf(h[i] + dppf<ROR4>(h[i])), oth = dppf<XOR1>(own);
      const float gt = odd ? oth : own, up = odd ? own : oth;
      h[i] = rbf(silu(gt)) * up;
    }
    m0 = pk(h[0], h[1]); m1 = pk(h[2], h[3]);
  }
  f32x4 y[5];
  const u32x4 b0 = build_b(m0, sel, 0), b1 = build_b(m0, sel, 1), b2 = build_b(m1, sel, 0), b3 = build_b(m1, sel, 1);
#pragma unroll
  for (int cb = 0; cb < 5; ++cb) y[cb] = mfma(b0, a[cb], f32x4{0.f, 0.f, 0.f, 0.f});
#pragma unroll
  for (int cb = 0; cb < 5; ++cb) y[cb] = mfma(b1, a[5 + cb], y[cb]);
  SBAR();
#pragma unroll
  for (int cb = 0; cb < 5; ++cb) y[cb] = mfma(b2, a[10 + cb], y[cb]);
  SBAR();
#pragma unroll
  for (int cb = 0; cb < 5; ++cb) { y[cb] = mfma(b3, a[15 + cb], y[cb]); SBAR(); }
#pragma unroll
  for (int cb = 0; cb < 5; ++cb) *(LAS f32x4*)((LAS char*)lds + yd + cb * 1280) = y[cb];
  wg_barrier();
  const LAS f32x4* p = (const LAS f32x4*)((LAS char*)lds + ys);
  const f32x4 t0 = p[0], t1 = p[400], t2 = p[800], t3 = p[1200], t4 = p[320], t5 = p[720], t6 = p[1120], t7 = p[1520];
  stsc(rw, po, pack4((t0 + t1) + (t2 + t3), tag));
  stsc(rw, po2, pack4((t4 + t5) + (t6 + t7), tag));
}

extern "C" void kernel_launch(void* const* d_in, const int* in_sizes, int n_in, void* d_out, int out_size, void* d_ws, size_t ws_size,
                              hipStream_t stream) {
  fused_decode<<<NWG, NTHR, 0, stream>>>((const u16*)d_in[1], (const u16*)d_in[2], (unsigned char*)d_ws, (const u16*)d_in[0], (const u16*)d_in[3],
                                          (const u16*)d_in[4], (const u16*)d_in[6], (const u16*)d_in[7], (const u16*)d_in[8], (u16*)d_out);
}
```
